```python
import torch

def _softmax(x, axis=-1):
    return torch.softmax(x, dim=axis)
def _gelu(x):
    return torch.nn.functional.gelu(x, approximate="tanh")
def _silu(x):
    return torch.nn.functional.silu(x)
def _sigmoid(x):
    return torch.sigmoid(x)
def _layernorm(x, axis=-1, eps=1e-5):
    m = x.mean(dim=axis, keepdim=True)
    v = x.var(dim=axis, keepdim=True, unbiased=False)
    return (x - m) / torch.sqrt(v + eps)
def _scatter_add(tgt, idx, src, axis=0):
    return tgt.clone().index_add_(axis, idx, src)
def _segment_sum(src, seg, n):
    out = torch.zeros((n,) + src.shape[1:], dtype=src.dtype)
    return out.index_add_(0, seg, src)
def _pad(x, pw):
    flat = tuple(v for pair in reversed(pw) for v in pair)
    return torch.nn.functional.pad(x, flat)

kernel_name = 'standalone__mha_softmax__near_ridge'

def setup_inputs():

    _w = (lambda a: (lambda t: t.to(torch.bfloat16) if t.is_floating_point() else t)(torch.as_tensor(a)))
    B, H, Sq, Sk, D = 8, 32, 384, 384, 128
    return {
        "q": _w((lambda *s: torch.randn(*s, dtype=torch.bfloat16) * 0.8)(B, H, Sq, D)),
        "k": _w((lambda *s: torch.randn(*s, dtype=torch.bfloat16) * 0.8)(B, H, Sk, D)),
        "v": _w((lambda *s: torch.randn(*s, dtype=torch.bfloat16) * 0.05)(B, H, Sk, D)),
    }

def reference(q, k, v):
    scale = q.shape[-1] ** -0.5
    scores = torch.einsum("bhqd,bhkd->bhqk", q, k) * scale
    p = _softmax(scores, axis=-1)
    return torch.einsum("bhqk,bhkd->bhqd", p, v)
```

```cpp
#include <hip/hip_runtime.h>
#include <hip/hip_bf16.h>
using bf16 = __hip_bfloat16;
constexpr int   D = 128, NW = 4, QBLK = 32, KVBLK = 64;
constexpr float SCALE = 0.088388347648318440f;
constexpr float THR = 8.f;
constexpr int B = 8, HQ = 32, S = 384;
constexpr int LDQ = D, LDK = D, LDO = D;
constexpr size_t SHM_V = KVBLK * D * 2, SHM_K = KVBLK * D * 2, SHM_ATTN = 2 * SHM_V + 2 * SHM_K + NW * 64 * 4;
using bf16x8 = __attribute__((ext_vector_type(8))) short;
using s16x4  = __attribute__((ext_vector_type(4))) short;
using f32x16 = __attribute__((ext_vector_type(16))) float;
using u32x4  = __attribute__((ext_vector_type(4))) unsigned;
#define KSWZ(row, colB) ((row) * 256 + ((colB) ^ (((row) & 7) << 4)))
#define SBAR() __builtin_amdgcn_sched_barrier(0)
__device__ __forceinline__ int crow(int r, int hi) { return (r & 3) + 8 * (r >> 2) + 4 * hi; }
__device__ __forceinline__ unsigned cvtpk(float lo, float hi) {
  unsigned r; asm volatile("v_cvt_pk_bf16_f32 %0, %1, %2" : "=v"(r) : "v"(lo), "v"(hi)); return r;
}
__device__ __forceinline__ bf16x8 ld8(const bf16* p) { return *reinterpret_cast<const bf16x8*>(p); }
__device__ __forceinline__ void partialSM(f32x16& p0, f32x16& p1, float& m_reg, float& mn, float& alpha) {
  constexpr float C = SCALE * 1.4426950408889634f;
  float pmax = p0[0]; for (int r = 1; r < 16; ++r) pmax = fmaxf(pmax, p0[r]); for (int r = 0; r < 16; ++r) pmax = fmaxf(pmax, p1[r]);
  { auto rr = __builtin_amdgcn_permlane32_swap(__float_as_uint(pmax), __float_as_uint(pmax), false, false);
    pmax = fmaxf(__uint_as_float(rr[0]), __uint_as_float(rr[1])); }
  if (__builtin_expect(__all(pmax - m_reg <= THR / SCALE), 1)) { mn = m_reg; alpha = 1.f; }
  else { mn = fmaxf(m_reg, pmax); alpha = __builtin_amdgcn_exp2f((m_reg - mn) * C); m_reg = mn; }
  float mnC = -mn * C;
  for (int r = 0; r < 16; ++r) p0[r] = fmaf(p0[r], C, mnC); for (int r = 0; r < 16; ++r) p1[r] = fmaf(p1[r], C, mnC);
  for (int r = 0; r < 16; ++r) p0[r] = __builtin_amdgcn_exp2f(p0[r]);
}
__device__ __forceinline__ void finishSM(f32x16& p0, f32x16& p1, float alpha, float& l_reg, bf16x8& pa0, bf16x8& pa1, bf16x8& pa2, bf16x8& pa3) {
  for (int r = 0; r < 16; ++r) p1[r] = __builtin_amdgcn_exp2f(p1[r]);
  float ps = 0; for (int r = 0; r < 16; ++r) ps += p0[r]; for (int r = 0; r < 16; ++r) ps += p1[r];
  { auto rr = __builtin_amdgcn_permlane32_swap(__float_as_uint(ps), __float_as_uint(ps), false, false);
    ps = __uint_as_float(rr[0]) + __uint_as_float(rr[1]); }
  l_reg = l_reg * alpha + ps;
#define PK4(P, BASE, OUT) do { unsigned a0 = cvtpk(P[BASE + 0], P[BASE + 1]), a1 = cvtpk(P[BASE + 2], P[BASE + 3]);   \
    unsigned b0 = cvtpk(P[BASE + 4], P[BASE + 5]), b1 = cvtpk(P[BASE + 6], P[BASE + 7]);                              \
    auto r0 = __builtin_amdgcn_permlane32_swap(a0, b0, false, false); auto r1 = __builtin_amdgcn_permlane32_swap(a1, b1, false, false); \
    u32x4 w = {r0[0], r1[0], r0[1], r1[1]}; OUT = *reinterpret_cast<bf16x8*>(&w); } while (0)
  PK4(p0, 0, pa0); PK4(p0, 8, pa1); PK4(p1, 0, pa2); PK4(p1, 8, pa3);
#undef PK4
}
__device__ __forceinline__ void qkt(f32x16& p0, f32x16& p1, const bf16* Ks, const bf16x8* qr, int r32, int hi) {
  p0 = f32x16{}; p1 = f32x16{};
  for (int d0 = 0; d0 < 8; ++d0) { int cb = (d0 * 16 + hi * 8) * 2;
    bf16x8 b0 = *reinterpret_cast<const bf16x8*>((const char*)Ks + KSWZ(r32, cb));
    bf16x8 b1 = *reinterpret_cast<const bf16x8*>((const char*)Ks + KSWZ(32 + r32, cb));
    p0 = __builtin_amdgcn_mfma_f32_32x32x16_bf16(b0, qr[d0], p0, 0, 0, 0);
    p1 = __builtin_amdgcn_mfma_f32_32x32x16_bf16(b1, qr[d0], p1, 0, 0, 0); }
}
__device__ __forceinline__ int v_st(int k, int c) { const int kk = (k & ~0xC) | ((k & 4) << 1) | ((k & 8) >> 1); return ((kk >> 3) * 4 + (c >> 5)) * 512 + ((kk & 7) * 32 + (c & 31)) * 2; }
__device__ __forceinline__ int v_rd_base(int lane) { return ((lane & 3) << 3) | (((lane >> 2) & 3) << 6) | (((lane >> 4) & 1) << 5) | (((lane >> 5) & 1) << 8); }
constexpr int v_rd_off(int d0, int ks, int half) { return d0 * 512 + ks * 4096 + half * 2048; }
template <int OFF> __device__ __forceinline__ s16x4 tr_read(int vb) {
  s16x4 r; asm volatile("ds_read_b64_tr_b16 %0, %1 offset:%2" : "=&v"(r) : "v"(vb), "i"(OFF) : "memory"); return r;
}
template <int D0> __device__ __forceinline__ void pv_one(f32x16& od, int vb, bf16x8 pa0, bf16x8 pa1, bf16x8 pa2, bf16x8 pa3) {
  const s16x4 l0 = tr_read<v_rd_off(D0, 0, 0)>(vb), h0 = tr_read<v_rd_off(D0, 0, 1)>(vb), l1 = tr_read<v_rd_off(D0, 1, 0)>(vb), h1 = tr_read<v_rd_off(D0, 1, 1)>(vb);
  const s16x4 l2 = tr_read<v_rd_off(D0, 2, 0)>(vb), h2 = tr_read<v_rd_off(D0, 2, 1)>(vb), l3 = tr_read<v_rd_off(D0, 3, 0)>(vb), h3 = tr_read<v_rd_off(D0, 3, 1)>(vb);
  asm volatile("s_waitcnt lgkmcnt(0)" ::: "memory"); SBAR();
#define PK(L, H) (bf16x8){L[0], L[1], L[2], L[3], H[0], H[1], H[2], H[3]}
  od = __builtin_amdgcn_mfma_f32_32x32x16_bf16(pa0, PK(l0, h0), od, 0, 0, 0);
  od = __builtin_amdgcn_mfma_f32_32x32x16_bf16(pa1, PK(l1, h1), od, 0, 0, 0);
  od = __builtin_amdgcn_mfma_f32_32x32x16_bf16(pa2, PK(l2, h2), od, 0, 0, 0);
  od = __builtin_amdgcn_mfma_f32_32x32x16_bf16(pa3, PK(l3, h3), od, 0, 0, 0);
#undef PK
}
__device__ __forceinline__ void pv_d0(f32x16* o, int vb, bf16x8 pa0, bf16x8 pa1, bf16x8 pa2, bf16x8 pa3) {
  pv_one<0>(o[0], vb, pa0, pa1, pa2, pa3); pv_one<1>(o[1], vb, pa0, pa1, pa2, pa3); pv_one<2>(o[2], vb, pa0, pa1, pa2, pa3); pv_one<3>(o[3], vb, pa0, pa1, pa2, pa3);
}
__device__ __forceinline__ void attn_dense_body(const bf16* __restrict__ Qb, const bf16* __restrict__ Kh, const bf16* __restrict__ Vh,
                                                bf16* __restrict__ Ob, int seq, char* lds) {
  const int tid = threadIdx.x, wid = tid >> 6, lane = tid & 63, r32 = lane & 31, hi = lane >> 5;
  bf16* V_lds = (bf16*)lds; bf16* K_lds = (bf16*)(lds + 2 * SHM_V);
  float* ws = (float*)(lds + 2 * SHM_V + 2 * SHM_K) + wid * 64; float* li_l = ws; float* al_l = ws + 32;
  float m_reg = -1e30f, l_reg = 0; f32x16 o[4] = {}; bf16x8 qr[8];
  const bf16* Qw = Qb + (long)(wid * QBLK + r32) * LDQ + hi * 8;
#pragma unroll
  for (int d0 = 0; d0 < 8; ++d0) qr[d0] = ld8(Qw + d0 * 16);
  const int sr = tid >> 4, sc = (tid & 15) * 8;
  const int vb0 = (int)(uintptr_t)V_lds + v_rd_base(lane);
  bf16x8 vs[4], ks[4];
#define SLOAD(k0) do { _Pragma("unroll") for (int i = 0; i < 4; ++i) { vs[i] = ld8(&Vh[(long)((k0) + 16 * i + sr) * LDK + sc]); ks[i] = ld8(&Kh[(long)((k0) + 16 * i + sr) * LDK + sc]); } } while (0)
#define SWRITE(b) do { _Pragma("unroll") for (int i = 0; i < 4; ++i) { *(bf16x8*)((char*)V_lds + (b) * SHM_V + v_st(16 * i + sr, sc)) = vs[i]; \
    *(bf16x8*)((char*)K_lds + (b) * SHM_K + KSWZ(16 * i + sr, sc * 2)) = ks[i]; } } while (0)
#define RESC(a) do { if (__any((a) < 1.f)) { if (hi == 0) al_l[r32] = (a); asm volatile("s_waitcnt lgkmcnt(0)" ::: "memory"); \
    for (int d = 0; d < 4; ++d) for (int r = 0; r < 16; ++r) o[d][r] *= al_l[crow(r, hi)]; } } while (0)
  f32x16 pA0, pA1, pB0, pB1; float mnA, mnB, alA, alB; bf16x8 pa0, pa1, pa2, pa3; const int NT = seq / KVBLK;
  SLOAD(0); SWRITE(0); __syncthreads();
  SLOAD(KVBLK);
  qkt(pA0, pA1, K_lds, qr, r32, hi); partialSM(pA0, pA1, m_reg, mnA, alA);
  SWRITE(1); __syncthreads();
  for (int j = 1; j + 1 < NT; j += 2) {
    SBAR(); qkt(pB0, pB1, (bf16*)((char*)K_lds + SHM_K), qr, r32, hi);
    finishSM(pA0, pA1, alA, l_reg, pa0, pa1, pa2, pa3); SBAR();
    SLOAD((j + 1) * KVBLK); SBAR();
    pv_d0(o, vb0, pa0, pa1, pa2, pa3); partialSM(pB0, pB1, m_reg, mnB, alB);
    __syncthreads(); SWRITE(0);
    RESC(alB); __syncthreads();
    SBAR(); qkt(pA0, pA1, K_lds, qr, r32, hi);
    finishSM(pB0, pB1, alB, l_reg, pa0, pa1, pa2, pa3); SBAR();
    SLOAD((j + 2) * KVBLK); SBAR();
    pv_d0(o, vb0 + (int)SHM_V, pa0, pa1, pa2, pa3); partialSM(pA0, pA1, m_reg, mnA, alA);
    __syncthreads(); SWRITE(1);
    RESC(alA); __syncthreads();
  }
  SBAR(); qkt(pB0, pB1, (bf16*)((char*)K_lds + SHM_K), qr, r32, hi);
  finishSM(pA0, pA1, alA, l_reg, pa0, pa1, pa2, pa3); SBAR();
  pv_d0(o, vb0, pa0, pa1, pa2, pa3); partialSM(pB0, pB1, m_reg, mnB, alB);
  __syncthreads(); RESC(alB);
  finishSM(pB0, pB1, alB, l_reg, pa0, pa1, pa2, pa3); SBAR();
  pv_d0(o, vb0 + (int)SHM_V, pa0, pa1, pa2, pa3);
  if (hi == 0) li_l[r32] = l_reg; asm volatile("s_waitcnt lgkmcnt(0)" ::: "memory");
  float rli[16];
#pragma unroll
  for (int r = 0; r < 16; ++r) rli[r] = __builtin_amdgcn_rcpf(li_l[crow(r, hi)]);
  bf16* Ow = Ob + (long)(wid * QBLK) * LDO;
#pragma unroll
  for (int r = 0; r < 16; ++r) { int orow = crow(r, hi);
    for (int d0 = 0; d0 < 4; ++d0) Ow[(long)orow * LDO + d0 * 32 + r32] = __float2bfloat16(o[d0][r] * rli[r]); }
}
__global__ __launch_bounds__(64 * NW, 2)
void attn_fwd_dense(const bf16* __restrict__ Q, const bf16* __restrict__ K, const bf16* __restrict__ V, bf16* __restrict__ O) {
  extern __shared__ __attribute__((aligned(16))) char shm[];
  const int id = blockIdx.x, x = id & 7, j = id >> 3;
  const long bh = x * 32 + j / 3, i0 = (long)(j % 3) * (QBLK * NW);
  const long q0 = (bh * S + i0) * D, k0 = bh * S * (long)D;
  attn_dense_body(Q + q0, K + k0, V + k0, O + q0, S, shm);
}
extern "C" void kernel_launch(void* const* d_in, const int* in_sizes, int n_in, void* d_out, int out_size, void* d_ws, size_t ws_size, hipStream_t stream) {
  (void)hipFuncSetAttribute((const void*)attn_fwd_dense, hipFuncAttributeMaxDynamicSharedMemorySize, (int)SHM_ATTN);
  hipLaunchKernelGGL(attn_fwd_dense, dim3(768), dim3(64 * NW), SHM_ATTN, stream,
                     (const bf16*)d_in[0], (const bf16*)d_in[1], (const bf16*)d_in[2], (bf16*)d_out);
}
```

```cpp
#include <hip/hip_runtime.h>
#include <hip/hip_bf16.h>

#define ATTN_STAMPS 0
#define ATTN_LEAK -1
#define DBGP ((unsigned*)0)
constexpr int kS = 384, kD = 128, kHeads = 256;
constexpr int kLds = 163840;

__device__ __forceinline__ float bf2f(unsigned short x) { return __uint_as_float((unsigned)x << 16); }

__device__ __forceinline__ void slow_row(const unsigned short* __restrict__ q, const unsigned short* __restrict__ K,
                                      const unsigned short* __restrict__ V, unsigned short* __restrict__ o, unsigned lane) {
  float s[6];
#pragma unroll
  for (int i = 0; i < 6; ++i) {
    const unsigned short* kr = K + (size_t)(lane + 64 * i) * kD;
    float acc = 0.f;
    for (int d = 0; d < kD; ++d) acc = fmaf(bf2f(q[d]), bf2f(kr[d]), acc);
    s[i] = acc * 0.12751743082f;
  }
  float m = fmaxf(fmaxf(fmaxf(s[0], s[1]), fmaxf(s[2], s[3])), fmaxf(s[4], s[5]));
  for (int off = 32; off; off >>= 1) m = fmaxf(m, __shfl_xor(m, off));
  float l = 0.f;
#pragma unroll
  for (int i = 0; i < 6; ++i) { s[i] = exp2f(s[i] - m); l += s[i]; }
  for (int off = 32; off; off >>= 1) l += __shfl_xor(l, off);
  float o0 = 0.f, o1 = 0.f;
#pragma unroll
  for (int i = 0; i < 6; ++i) {
    for (int j = 0; j < 64; ++j) {
      const float pj = __shfl(s[i], j);
      const unsigned vv = *reinterpret_cast<const unsigned*>(V + (size_t)(j + 64 * i) * kD + 2 * lane);
      o0 = fmaf(pj, __uint_as_float(vv << 16), o0);
      o1 = fmaf(pj, __uint_as_float(vv & 0xffff0000u), o1);
    }
  }
  const float r = 1.0f / l;
  const __hip_bfloat16 b0 = __float2bfloat16(o0 * r), b1 = __float2bfloat16(o1 * r);
  o[2 * lane] = *reinterpret_cast<const unsigned short*>(&b0);
  o[2 * lane + 1] = *reinterpret_cast<const unsigned short*>(&b1);
}

__global__ __launch_bounds__(256, 1) void mha_fwd_kernel(const unsigned short* Q, const unsigned short* K,
                                                         const unsigned short* V, unsigned short* O
) {
  extern __shared__ __attribute__((aligned(16))) char lds_mem[];
  const unsigned w = __builtin_amdgcn_readfirstlane(threadIdx.x >> 6);
  const size_t hb = (size_t)blockIdx.x * (kS * kD);
  const size_t qp = (size_t)(Q + hb), kp = (size_t)(K + hb), vp = (size_t)(V + hb), op = (size_t)(O + hb);
  const unsigned ldsb = (unsigned)(size_t)lds_mem;
  unsigned m0, m1, m2, m3, m4, m5;
  asm volatile(
    "s_mov_b32 s36, %[qlo]\n\t"
    "s_and_b32 s37, %[qhi], 0xffff\n\t"
    "s_mov_b32 s38, 98304\n\t"
    "s_mov_b32 s39, 0x20000\n\t"
    "s_mul_i32 s55, %[w], 24576\n\t"
    "s_lshl_b32 s59, %[w], 13\n\t"
    "s_add_u32 s59, s59, %[lds]\n\t"
    "s_add_u32 s56, s59, 131072\n\t"
    "s_add_u32 s57, s59, 32768\n\t"
    "v_mbcnt_lo_u32_b32 v240, -1, 0\n\t"
    "v_mbcnt_hi_u32_b32 v240, -1, v240\n\t"
    "v_lshrrev_b32 v241, 4, v240\n\t"
    "v_and_b32 v242, 7, v240\n\t"
    "v_xor_b32 v242, v241, v242\n\t"
    "v_lshlrev_b32 v242, 4, v242\n\t"
    "v_lshrrev_b32 v243, 3, v240\n\t"
    "v_lshl_add_u32 v51, v243, 8, v242\n\t"
    "v_xor_b32 v52, 64, v51\n\t"
    "s_add_u32 m0, s56, 0\n\t"
    "s_add_u32 s58, s55, 0\n\t"
    "buffer_load_dwordx4 v51, s[36:39], s58 offen nt lds\n\t"
    "buffer_load_dwordx4 v52, s[36:39], s58 offen offset:2048 nt lds\n\t"
    "s_add_u32 m0, s56, 1024\n\t"
    "s_add_u32 s58, s55, 128\n\t"
    "buffer_load_dwordx4 v51, s[36:39], s58 offen nt lds\n\t"
    "buffer_load_dwordx4 v52, s[36:39], s58 offen offset:2048 nt lds\n\t"
    "s_add_u32 m0, s56, 4096\n\t"
    "s_add_u32 s58, s55, 4096\n\t"
    "buffer_load_dwordx4 v51, s[36:39], s58 offen nt lds\n\t"
    "buffer_load_dwordx4 v52, s[36:39], s58 offen offset:2048 nt lds\n\t"
    "s_add_u32 m0, s56, 5120\n\t"
    "s_add_u32 s58, s55, 4224\n\t"
    "buffer_load_dwordx4 v51, s[36:39], s58 offen nt lds\n\t"
    "buffer_load_dwordx4 v52, s[36:39], s58 offen offset:2048 nt lds\n\t"
    "s_mov_b32 s40, %[klo]\n\t"
    "s_and_b32 s41, %[khi], 0xffff\n\t"
    "s_mov_b32 s42, 98304\n\t"
    "s_mov_b32 s43, 0x20000\n\t"
    "s_mov_b32 s44, %[vlo]\n\t"
    "s_and_b32 s45, %[vhi], 0xffff\n\t"
    "s_mov_b32 s46, 98304\n\t"
    "s_mov_b32 s47, 0x20000\n\t"
    "s_lshr_b32 s59, %[w], 1\n\t"
    "s_lshl_b32 s59, s59, 12\n\t"
    "s_and_b32 s72, %[w], 1\n\t"
    "s_lshl_b32 s52, s72, 7\n\t"
    "s_add_u32 s52, s52, s59\n\t"
    "s_lshl_b32 s53, s72, 10\n\t"
    "s_add_u32 s53, s53, s59\n\t"
    "s_add_u32 s53, s53, %[lds]\n\t"
    "s_add_u32 s54, s53, 65536\n\t"
    "v_bfe_u32 v242, v240, 1, 3\n\t"
    "v_lshlrev_b32 v242, 8, v242\n\t"
    "v_lshl_add_u32 v242, v241, 5, v242\n\t"
    "v_and_b32 v243, 1, v240\n\t"
    "v_lshl_add_u32 v53, v243, 4, v242\n\t"
    "s_add_u32 m0, s53, 0\n\t"
    "s_add_u32 s58, s52, 0\n\t"
    "buffer_load_dwordx4 v51, s[40:43], s58 offen nt lds\n\t"
    "buffer_load_dwordx4 v52, s[40:43], s58 offen offset:2048 nt lds\n\t"
    "s_add_u32 m0, s54, 0\n\t"
    "s_nop 0\n\t"
    "buffer_load_dwordx4 v53, s[44:47], s58 offen nt lds\n\t"
    "buffer_load_dwordx4 v53, s[44:47], s58 offen offset:2048 nt lds\n\t"
    "s_add_u32 m0, s57, 0\n\t"
    "s_add_u32 s58, s55, 8192\n\t"
    "buffer_load_dwordx4 v51, s[36:39], s58 offen nt lds\n\t"
    "buffer_load_dwordx4 v52, s[36:39], s58 offen offset:2048 nt lds\n\t"
    "s_add_u32 m0, s57, 1024\n\t"
    "s_add_u32 s58, s55, 8320\n\t"
    "buffer_load_dwordx4 v51, s[36:39], s58 offen nt lds\n\t"
    "buffer_load_dwordx4 v52, s[36:39], s58 offen offset:2048 nt lds\n\t"
    "v_lshlrev_b32 v54, 4, v240\n\t"
    "s_getpc_b64 s[32:33]\n\t"
    "s_and_b32 s33, s33, 0xffff\n\t"
    "s_mov_b32 s34, 31000\n\t"
    "s_mov_b32 s35, 0x20000\n\t"
    "s_lshl_b32 s31, %[xi], 10\n\t"
    "buffer_load_dwordx4 v[252:255], v54, s[32:35], s31 offen\n\t"
    "s_mov_b32 s48, %[olo]\n\t"
    "s_and_b32 s49, %[ohi], 0xffff\n\t"
    "s_mov_b32 s50, 98304\n\t"
    "s_mov_b32 s51, 0x20000\n\t"
    "s_mov_b32 s73, 0x3e0293ee\n\t"
    "s_mov_b32 s74, 0xffff0000\n\t"
    "s_mov_b32 s72, 0x53800000\n\t"
    "v_bfe_u32 v242, v240, 1, 3\n\t"
    "v_xor_b32 v242, v241, v242\n\t"
    "v_lshlrev_b32 v242, 4, v242\n\t"
    "v_and_b32 v243, 7, v240\n\t"
    "v_lshl_add_u32 v242, v243, 7, v242\n\t"
    "v_bfe_u32 v243, v240, 3, 1\n\t"
    "v_lshl_add_u32 v242, v243, 11, v242\n\t"
    "v_xor_b32 v243, 64, v242\n\t"
    "v_add_u32 v48, %[lds], v242\n\t"
    "v_add_u32 v49, %[lds], v243\n\t"
    "v_lshrrev_b32 v242, 5, v240\n\t"
    "v_lshlrev_b32 v242, 11, v242\n\t"
    "v_bfe_u32 v243, v240, 4, 1\n\t"
    "v_lshl_add_u32 v242, v243, 7, v242\n\t"
    "v_bfe_u32 v243, v240, 2, 2\n\t"
    "v_lshl_add_u32 v242, v243, 5, v242\n\t"
    "v_and_b32 v243, 3, v240\n\t"
    "v_lshl_add_u32 v242, v243, 3, v242\n\t"
    "v_add_u32 v242, %[lds], v242\n\t"
    "v_add_u32 v50, 65536, v242\n\t"
    "v_and_b32 v244, 15, v240\n\t"
    "v_lshrrev_b32 v242, 5, v240\n\t"
    "v_and_b32 v243, 7, v240\n\t"
    "v_xor_b32 v242, v243, v242\n\t"
    "v_lshlrev_b32 v242, 4, v242\n\t"
    "v_lshl_add_u32 v242, v244, 8, v242\n\t"
    "v_bfe_u32 v243, v240, 4, 1\n\t"
    "v_lshl_add_u32 v242, v243, 3, v242\n\t"
    "v_xor_b32 v243, 0, v242\n\t"
    "v_add_u32 v55, s56, v243\n\t"
    "v_xor_b32 v243, 32, v242\n\t"
    "v_add_u32 v56, s56, v243\n\t"
    "v_xor_b32 v243, 64, v242\n\t"
    "v_add_u32 v57, s56, v243\n\t"
    "v_xor_b32 v243, 96, v242\n\t"
    "v_add_u32 v58, s56, v243\n\t"
    "v_xor_b32 v242, v241, v244\n\t"
    "v_lshlrev_b32 v242, 4, v242\n\t"
    "v_lshl_add_u32 v242, v241, 8, v242\n\t"
    "v_xor_b32 v243, 64, v242\n\t"
    "v_add_u32 v59, s56, v242\n\t"
    "v_add_u32 v60, s56, v243\n\t"
    "v_and_b32 v242, 3, v240\n\t"
    "v_mov_b32 v243, 0x3f803f80\n\t"
    "v_cmp_eq_u32 vcc, 0, v242\n\t"
    "s_nop 1\n\t"
    "v_cndmask_b32 v243, 0, v243, vcc\n\t"
    "v_accvgpr_write_b32 a184, v243\n\t"
    "v_accvgpr_write_b32 a185, v243\n\t"
    "v_accvgpr_write_b32 a186, v243\n\t"
    "v_accvgpr_write_b32 a187, v243\n\t"
    "s_sub_u32 s59, s56, %[lds]\n\t"
    "v_add_u32 v61, s59, v48\n\t"
    "v_add_u32 v62, s59, v49\n\t"
    "s_waitcnt vmcnt(15)\n\t"
    "ds_read_b128 v[64:67], v61 offset:0\n\t"
    "ds_read_b128 v[68:71], v62 offset:0\n\t"
    "s_waitcnt lgkmcnt(1)\n\t"
    "v_lshlrev_b32 v72, 16, v64\n\t"
    "v_and_b32 v73, s74, v64\n\t"
    "v_mul_f32 v72, s73, v72\n\t"
    "v_mul_f32 v73, s73, v73\n\t"
    "v_cvt_pk_bf16_f32 v72, v72, v73\n\t"
    "v_accvgpr_write_b32 a0, v72\n\t"
    "v_lshlrev_b32 v74, 16, v65\n\t"
    "v_and_b32 v75, s74, v65\n\t"
    "v_mul_f32 v74, s73, v74\n\t"
    "v_mul_f32 v75, s73, v75\n\t"
    "v_cvt_pk_bf16_f32 v74, v74, v75\n\t"
    "v_accvgpr_write_b32 a1, v74\n\t"
    "v_lshlrev_b32 v76, 16, v66\n\t"
    "v_and_b32 v77, s74, v66\n\t"
    "v_mul_f32 v76, s73, v76\n\t"
    "v_mul_f32 v77, s73, v77\n\t"
    "v_cvt_pk_bf16_f32 v76, v76, v77\n\t"
    "v_accvgpr_write_b32 a2, v76\n\t"
    "v_lshlrev_b32 v78, 16, v67\n\t"
    "v_and_b32 v79, s74, v67\n\t"
    "v_mul_f32 v78, s73, v78\n\t"
    "v_mul_f32 v79, s73, v79\n\t"
    "v_cvt_pk_bf16_f32 v78, v78, v79\n\t"
    "v_accvgpr_write_b32 a3, v78\n\t"
    "s_waitcnt lgkmcnt(0)\n\t"
    "v_lshlrev_b32 v80, 16, v68\n\t"
    "v_and_b32 v81, s74, v68\n\t"
    "v_mul_f32 v80, s73, v80\n\t"
    "v_mul_f32 v81, s73, v81\n\t"
    "v_cvt_pk_bf16_f32 v80, v80, v81\n\t"
    "v_accvgpr_write_b32 a4, v80\n\t"
    "v_lshlrev_b32 v82, 16, v69\n\t"
    "v_and_b32 v83, s74, v69\n\t"
    "v_mul_f32 v82, s73, v82\n\t"
    "v_mul_f32 v83, s73, v83\n\t"
    "v_cvt_pk_bf16_f32 v82, v82, v83\n\t"
    "v_accvgpr_write_b32 a5, v82\n\t"
    "v_lshlrev_b32 v84, 16, v70\n\t"
    "v_and_b32 v85, s74, v70\n\t"
    "v_mul_f32 v84, s73, v84\n\t"
    "v_mul_f32 v85, s73, v85\n\t"
    "v_cvt_pk_bf16_f32 v84, v84, v85\n\t"
    "v_accvgpr_write_b32 a6, v84\n\t"
    "v_lshlrev_b32 v86, 16, v71\n\t"
    "v_and_b32 v87, s74, v71\n\t"
    "v_mul_f32 v86, s73, v86\n\t"
    "v_mul_f32 v87, s73, v87\n\t"
    "v_cvt_pk_bf16_f32 v86, v86, v87\n\t"
    "v_accvgpr_write_b32 a7, v86\n\t"
    "s_waitcnt vmcnt(13)\n\t"
    "ds_read_b128 v[88:91], v61 offset:1024\n\t"
    "ds_read_b128 v[92:95], v62 offset:1024\n\t"
    "s_add_u32 m0, s57, 4096\n\t"
    "s_add_u32 s58, s55, 12288\n\t"
    "buffer_load_dwordx4 v51, s[36:39], s58 offen nt lds\n\t"
    "buffer_load_dwordx4 v52, s[36:39], s58 offen offset:2048 nt lds\n\t"
    "s_waitcnt lgkmcnt(1)\n\t"
    "v_lshlrev_b32 v72, 16, v88\n\t"
    "v_and_b32 v73, s74, v88\n\t"
    "v_mul_f32 v72, s73, v72\n\t"
    "v_mul_f32 v73, s73, v73\n\t"
    "v_cvt_pk_bf16_f32 v72, v72, v73\n\t"
    "v_accvgpr_write_b32 a8, v72\n\t"
    "v_lshlrev_b32 v74, 16, v89\n\t"
    "v_and_b32 v75, s74, v89\n\t"
    "v_mul_f32 v74, s73, v74\n\t"
    "v_mul_f32 v75, s73, v75\n\t"
    "v_cvt_pk_bf16_f32 v74, v74, v75\n\t"
    "v_accvgpr_write_b32 a9, v74\n\t"
    "v_lshlrev_b32 v76, 16, v90\n\t"
    "v_and_b32 v77, s74, v90\n\t"
    "v_mul_f32 v76, s73, v76\n\t"
    "v_mul_f32 v77, s73, v77\n\t"
    "v_cvt_pk_bf16_f32 v76, v76, v77\n\t"
    "v_accvgpr_write_b32 a10, v76\n\t"
    "v_lshlrev_b32 v78, 16, v91\n\t"
    "v_and_b32 v79, s74, v91\n\t"
    "v_mul_f32 v78, s73, v78\n\t"
    "v_mul_f32 v79, s73, v79\n\t"
    "v_cvt_pk_bf16_f32 v78, v78, v79\n\t"
    "v_accvgpr_write_b32 a11, v78\n\t"
    "s_waitcnt lgkmcnt(0)\n\t"
    "v_lshlrev_b32 v80, 16, v92\n\t"
    "v_and_b32 v81, s74, v92\n\t"
    "v_mul_f32 v80, s73, v80\n\t"
    "v_mul_f32 v81, s73, v81\n\t"
    "v_cvt_pk_bf16_f32 v80, v80, v81\n\t"
    "v_accvgpr_write_b32 a12, v80\n\t"
    "v_lshlrev_b32 v82, 16, v93\n\t"
    "v_and_b32 v83, s74, v93\n\t"
    "v_mul_f32 v82, s73, v82\n\t"
    "v_mul_f32 v83, s73, v83\n\t"
    "v_cvt_pk_bf16_f32 v82, v82, v83\n\t"
    "v_accvgpr_write_b32 a13, v82\n\t"
    "v_lshlrev_b32 v84, 16, v94\n\t"
    "v_and_b32 v85, s74, v94\n\t"
    "v_mul_f32 v84, s73, v84\n\t"
    "v_mul_f32 v85, s73, v85\n\t"
    "v_cvt_pk_bf16_f32 v84, v84, v85\n\t"
    "v_accvgpr_write_b32 a14, v84\n\t"
    "v_lshlrev_b32 v86, 16, v95\n\t"
    "v_and_b32 v87, s74, v95\n\t"
    "v_mul_f32 v86, s73, v86\n\t"
    "v_mul_f32 v87, s73, v87\n\t"
    "v_cvt_pk_bf16_f32 v86, v86, v87\n\t"
    "v_accvgpr_write_b32 a15, v86\n\t"
    "s_waitcnt vmcnt(13)\n\t"
    "ds_read_b128 v[96:99], v61 offset:4096\n\t"
    "ds_read_b128 v[100:103], v62 offset:4096\n\t"
    "s_add_u32 m0, s57, 5120\n\t"
    "s_add_u32 s58, s55, 12416\n\t"
    "buffer_load_dwordx4 v51, s[36:39], s58 offen nt lds\n\t"
    "buffer_load_dwordx4 v52, s[36:39], s58 offen offset:2048 nt lds\n\t"
    "s_waitcnt lgkmcnt(1)\n\t"
    "v_lshlrev_b32 v104, 16, v96\n\t"
    "v_and_b32 v105, s74, v96\n\t"
    "v_mul_f32 v104, s73, v104\n\t"
    "v_mul_f32 v105, s73, v105\n\t"
    "v_cvt_pk_bf16_f32 v104, v104, v105\n\t"
    "v_accvgpr_write_b32 a16, v104\n\t"
    "v_lshlrev_b32 v106, 16, v97\n\t"
    "v_and_b32 v107, s74, v97\n\t"
    "v_mul_f32 v106, s73, v106\n\t"
    "v_mul_f32 v107, s73, v107\n\t"
    "v_cvt_pk_bf16_f32 v106, v106, v107\n\t"
    "v_accvgpr_write_b32 a17, v106\n\t"
    "v_lshlrev_b32 v108, 16, v98\n\t"
    "v_and_b32 v109, s74, v98\n\t"
    "v_mul_f32 v108, s73, v108\n\t"
    "v_mul_f32 v109, s73, v109\n\t"
    "v_cvt_pk_bf16_f32 v108, v108, v109\n\t"
    "v_accvgpr_write_b32 a18, v108\n\t"
    "v_lshlrev_b32 v110, 16, v99\n\t"
    "v_and_b32 v111, s74, v99\n\t"
    "v_mul_f32 v110, s73, v110\n\t"
    "v_mul_f32 v111, s73, v111\n\t"
    "v_cvt_pk_bf16_f32 v110, v110, v111\n\t"
    "v_accvgpr_write_b32 a19, v110\n\t"
    "s_waitcnt lgkmcnt(0)\n\t"
    "v_lshlrev_b32 v112, 16, v100\n\t"
    "v_and_b32 v113, s74, v100\n\t"
    "v_mul_f32 v112, s73, v112\n\t"
    "v_mul_f32 v113, s73, v113\n\t"
    "v_cvt_pk_bf16_f32 v112, v112, v113\n\t"
    "v_accvgpr_write_b32 a20, v112\n\t"
    "v_lshlrev_b32 v114, 16, v101\n\t"
    "v_and_b32 v115, s74, v101\n\t"
    "v_mul_f32 v114, s73, v114\n\t"
    "v_mul_f32 v115, s73, v115\n\t"
    "v_cvt_pk_bf16_f32 v114, v114, v115\n\t"
    "v_accvgpr_write_b32 a21, v114\n\t"
    "v_lshlrev_b32 v116, 16, v102\n\t"
    "v_and_b32 v117, s74, v102\n\t"
    "v_mul_f32 v116, s73, v116\n\t"
    "v_mul_f32 v117, s73, v117\n\t"
    "v_cvt_pk_bf16_f32 v116, v116, v117\n\t"
    "v_accvgpr_write_b32 a22, v116\n\t"
    "v_lshlrev_b32 v118, 16, v103\n\t"
    "v_and_b32 v119, s74, v103\n\t"
    "v_mul_f32 v118, s73, v118\n\t"
    "v_mul_f32 v119, s73, v119\n\t"
    "v_cvt_pk_bf16_f32 v118, v118, v119\n\t"
    "v_accvgpr_write_b32 a23, v118\n\t"
    "s_waitcnt vmcnt(13)\n\t"
    "ds_read_b128 v[120:123], v61 offset:5120\n\t"
    "ds_read_b128 v[124:127], v62 offset:5120\n\t"
    "s_add_u32 m0, s56, 0\n\t"
    "s_add_u32 s58, s55, 16384\n\t"
    "buffer_load_dwordx4 v51, s[36:39], s58 offen nt lds\n\t"
    "buffer_load_dwordx4 v52, s[36:39], s58 offen offset:2048 nt lds\n\t"
    "s_waitcnt lgkmcnt(1)\n\t"
    "v_lshlrev_b32 v104, 16, v120\n\t"
    "v_and_b32 v105, s74, v120\n\t"
    "v_mul_f32 v104, s73, v104\n\t"
    "v_mul_f32 v105, s73, v105\n\t"
    "v_cvt_pk_bf16_f32 v104, v104, v105\n\t"
    "v_accvgpr_write_b32 a24, v104\n\t"
    "v_lshlrev_b32 v106, 16, v121\n\t"
    "v_and_b32 v107, s74, v121\n\t"
    "v_mul_f32 v106, s73, v106\n\t"
    "v_mul_f32 v107, s73, v107\n\t"
    "v_cvt_pk_bf16_f32 v106, v106, v107\n\t"
    "v_accvgpr_write_b32 a25, v106\n\t"
    "v_lshlrev_b32 v108, 16, v122\n\t"
    "v_and_b32 v109, s74, v122\n\t"
    "v_mul_f32 v108, s73, v108\n\t"
    "v_mul_f32 v109, s73, v109\n\t"
    "v_cvt_pk_bf16_f32 v108, v108, v109\n\t"
    "v_accvgpr_write_b32 a26, v108\n\t"
    "v_lshlrev_b32 v110, 16, v123\n\t"
    "v_and_b32 v111, s74, v123\n\t"
    "v_mul_f32 v110, s73, v110\n\t"
    "v_mul_f32 v111, s73, v111\n\t"
    "v_cvt_pk_bf16_f32 v110, v110, v111\n\t"
    "v_accvgpr_write_b32 a27, v110\n\t"
    "s_waitcnt lgkmcnt(0)\n\t"
    "v_lshlrev_b32 v112, 16, v124\n\t"
    "v_and_b32 v113, s74, v124\n\t"
    "v_mul_f32 v112, s73, v112\n\t"
    "v_mul_f32 v113, s73, v113\n\t"
    "v_cvt_pk_bf16_f32 v112, v112, v113\n\t"
    "v_accvgpr_write_b32 a28, v112\n\t"
    "v_lshlrev_b32 v114, 16, v125\n\t"
    "v_and_b32 v115, s74, v125\n\t"
    "v_mul_f32 v114, s73, v114\n\t"
    "v_mul_f32 v115, s73, v115\n\t"
    "v_cvt_pk_bf16_f32 v114, v114, v115\n\t"
    "v_accvgpr_write_b32 a29, v114\n\t"
    "v_lshlrev_b32 v116, 16, v126\n\t"
    "v_and_b32 v117, s74, v126\n\t"
    "v_mul_f32 v116, s73, v116\n\t"
    "v_mul_f32 v117, s73, v117\n\t"
    "v_cvt_pk_bf16_f32 v116, v116, v117\n\t"
    "v_accvgpr_write_b32 a30, v116\n\t"
    "v_lshlrev_b32 v118, 16, v127\n\t"
    "v_and_b32 v119, s74, v127\n\t"
    "v_mul_f32 v118, s73, v118\n\t"
    "v_mul_f32 v119, s73, v119\n\t"
    "v_cvt_pk_bf16_f32 v118, v118, v119\n\t"
    "v_accvgpr_write_b32 a31, v118\n\t"
    "s_waitcnt vmcnt(11)\n\t"
    "s_barrier\n\t"
    "s_add_u32 m0, s56, 1024\n\t"
    "s_add_u32 s58, s55, 16512\n\t"
    "buffer_load_dwordx4 v51, s[36:39], s58 offen nt lds\n\t"
    "buffer_load_dwordx4 v52, s[36:39], s58 offen offset:2048 nt lds\n\t"
    "s_add_u32 m0, s56, 4096\n\t"
    "s_add_u32 s58, s55, 20480\n\t"
    "buffer_load_dwordx4 v51, s[36:39], s58 offen nt lds\n\t"
    "buffer_load_dwordx4 v52, s[36:39], s58 offen offset:2048 nt lds\n\t"
    "ds_read_b128 a[96:99], v48 offset:0\n\t"
    "ds_read_b128 a[100:103], v49 offset:0\n\t"
    "ds_read_b128 a[104:107], v48 offset:1024\n\t"
    "ds_read_b128 a[108:111], v49 offset:1024\n\t"
    "ds_read_b128 a[112:115], v48 offset:4096\n\t"
    "ds_read_b128 a[116:119], v49 offset:4096\n\t"
    "ds_read_b128 a[120:123], v48 offset:5120\n\t"
    "ds_read_b128 a[124:127], v49 offset:5120\n\t"
    "s_waitcnt lgkmcnt(7)\n\t"
    "v_mfma_f32_16x16x32_bf16 v[0:3], a[96:99], a[0:3], 0\n\t"
    "ds_read_b64_tr_b16 a[128:129], v50 offset:0\n\t"
    "ds_read_b64_tr_b16 a[130:131], v50 offset:4096\n\t"
    "s_waitcnt lgkmcnt(8)\n\t"
    "v_mfma_f32_16x16x32_bf16 v[0:3], a[100:103], a[4:7], v[0:3]\n\t"
    "ds_read_b64_tr_b16 a[132:133], v50 offset:256\n\t"
    "ds_read_b64_tr_b16 a[134:135], v50 offset:4352\n\t"
    "s_waitcnt lgkmcnt(9)\n\t"
    "v_mfma_f32_16x16x32_bf16 v[0:3], a[104:107], a[8:11], v[0:3]\n\t"
    "ds_read_b64_tr_b16 a[136:137], v50 offset:512\n\t"
    "ds_read_b64_tr_b16 a[138:139], v50 offset:4608\n\t"
    "s_waitcnt lgkmcnt(10)\n\t"
    "v_mfma_f32_16x16x32_bf16 v[0:3], a[108:111], a[12:15], v[0:3]\n\t"
    "ds_read_b64_tr_b16 a[140:141], v50 offset:768\n\t"
    "ds_read_b64_tr_b16 a[142:143], v50 offset:4864\n\t"
    "s_waitcnt lgkmcnt(11)\n\t"
    "v_mfma_f32_16x16x32_bf16 v[4:7], a[112:115], a[0:3], 0\n\t"
    "ds_read_b64_tr_b16 a[144:145], v50 offset:1024\n\t"
    "ds_read_b64_tr_b16 a[146:147], v50 offset:5120\n\t"
    "s_waitcnt lgkmcnt(12)\n\t"
    "v_mfma_f32_16x16x32_bf16 v[4:7], a[116:119], a[4:7], v[4:7]\n\t"
    "ds_read_b64_tr_b16 a[148:149], v50 offset:1280\n\t"
    "ds_read_b64_tr_b16 a[150:151], v50 offset:5376\n\t"
    "s_waitcnt lgkmcnt(13)\n\t"
    "v_mfma_f32_16x16x32_bf16 v[4:7], a[120:123], a[8:11], v[4:7]\n\t"
    "ds_read_b64_tr_b16 a[152:153], v50 offset:1536\n\t"
    "v_max3_f32 v94, v0, v1, v2\n\t"
    "s_waitcnt lgkmcnt(13)\n\t"
    "v_mfma_f32_16x16x32_bf16 v[4:7], a[124:127], a[12:15], v[4:7]\n\t"
    "ds_read_b64_tr_b16 a[154:155], v50 offset:5632\n\t"
    "ds_read_b64_tr_b16 a[156:157], v50 offset:1792\n\t"
    "v_mfma_f32_16x16x32_bf16 v[8:11], a[96:99], a[16:19], 0\n\t"
    "ds_read_b64_tr_b16 a[158:159], v50 offset:5888\n\t"
    "s_add_u32 s75, s55, 0\n\t"
    "v_mfma_f32_16x16x32_bf16 v[8:11], a[100:103], a[20:23], v[8:11]\n\t"
    "s_add_u32 s31, s55, 4096\n\t"
    "v_mfma_f32_16x16x32_bf16 v[8:11], a[104:107], a[24:27], v[8:11]\n\t"
    "v_max3_f32 v95, v3, v4, v5\n\t"
    "v_max3_f32 v94, v94, v6, v7\n\t"
    "v_mfma_f32_16x16x32_bf16 v[8:11], a[108:111], a[28:31], v[8:11]\n\t"
    "v_max_f32 v94, v94, v95\n\t"
    "v_mov_b32 v95, v94\n\t"
    "v_mfma_f32_16x16x32_bf16 v[12:15], a[112:115], a[16:19], 0\n\t"
    "v_mfma_f32_16x16x32_bf16 v[12:15], a[116:119], a[20:23], v[12:15]\n\t"
    "v_permlane32_swap_b32 v94, v95\n\t"
    "v_max_f32 v94, v94, v95\n\t"
    "v_mov_b32 v95, v94\n\t"
    "v_mfma_f32_16x16x32_bf16 v[12:15], a[120:123], a[24:27], v[12:15]\n\t"
    "v_max3_f32 v126, v8, v9, v10\n\t"
    "v_permlane16_swap_b32 v94, v95\n\t"
    "v_max_f32 v94, v94, v95\n\t"
    "v_sub_f32 v0, v0, v94\n\t"
    "v_sub_f32 v1, v1, v94\n\t"
    "v_sub_f32 v2, v2, v94\n\t"
    "v_mfma_f32_16x16x32_bf16 v[12:15], a[124:127], a[28:31], v[12:15]\n\t"
    "v_sub_f32 v3, v3, v94\n\t"
    "v_sub_f32 v4, v4, v94\n\t"
    "v_sub_f32 v5, v5, v94\n\t"
    "v_sub_f32 v6, v6, v94\n\t"
    "v_sub_f32 v7, v7, v94\n\t"
    "v_exp_f32 v0, v0\n\t"
    "v_exp_f32 v1, v1\n\t"
    "v_exp_f32 v2, v2\n\t"
    "v_exp_f32 v3, v3\n\t"
    "v_exp_f32 v4, v4\n\t"
    "v_exp_f32 v5, v5\n\t"
    "v_exp_f32 v6, v6\n\t"
    "v_exp_f32 v7, v7\n\t"
    "v_cvt_pk_bf16_f32 v16, v0, v1\n\t"
    "v_cvt_pk_bf16_f32 v17, v2, v3\n\t"
    "v_cvt_pk_bf16_f32 v18, v4, v5\n\t"
    "v_cvt_pk_bf16_f32 v19, v6, v7\n\t"
    "s_waitcnt lgkmcnt(0)\n\t"
    "s_nop 0\n\t"
    "v_mfma_f32_16x16x32_bf16 v[64:67], a[128:131], v[16:19], 0\n\t"
    "v_max3_f32 v127, v11, v12, v13\n\t"
    "v_max3_f32 v126, v126, v14, v15\n\t"
    "v_mfma_f32_16x16x32_bf16 v[68:71], a[132:135], v[16:19], 0\n\t"
    "v_max_f32 v126, v126, v127\n\t"
    "v_mov_b32 v127, v126\n\t"
    "v_mfma_f32_16x16x32_bf16 v[72:75], a[136:139], v[16:19], 0\n\t"
    "v_sub_f32 v24, 0, v94\n\t"
    "v_sub_f32 v25, 0, v94\n\t"
    "v_mfma_f32_16x16x32_bf16 v[76:79], a[140:143], v[16:19], 0\n\t"
    "v_sub_f32 v26, 0, v94\n\t"
    "v_sub_f32 v27, 0, v94\n\t"
    "v_mfma_f32_16x16x32_bf16 v[80:83], a[144:147], v[16:19], 0\n\t"
    "v_permlane32_swap_b32 v126, v127\n\t"
    "v_max_f32 v126, v126, v127\n\t"
    "v_mfma_f32_16x16x32_bf16 v[84:87], a[148:151], v[16:19], 0\n\t"
    "v_mov_b32 v127, v126\n\t"
    "v_mfma_f32_16x16x32_bf16 v[88:91], a[152:155], v[16:19], 0\n\t"
    "v_mfma_f32_16x16x32_bf16 v[92:95], a[156:159], v[16:19], 0\n\t"
    "v_permlane16_swap_b32 v126, v127\n\t"
    "v_max_f32 v126, v126, v127\n\t"
    "v_sub_f32 v8, v8, v126\n\t"
    "v_sub_f32 v9, v9, v126\n\t"
    "v_sub_f32 v10, v10, v126\n\t"
    "v_sub_f32 v11, v11, v126\n\t"
    "v_mfma_f32_16x16x32_bf16 a[160:163], a[184:187], v[16:19], 0\n\t"
    "v_sub_f32 v12, v12, v126\n\t"
    "v_sub_f32 v13, v13, v126\n\t"
    "v_sub_f32 v14, v14, v126\n\t"
    "v_sub_f32 v15, v15, v126\n\t"
    "v_exp_f32 v8, v8\n\t"
    "v_exp_f32 v9, v9\n\t"
    "v_exp_f32 v10, v10\n\t"
    "v_exp_f32 v11, v11\n\t"
    "v_exp_f32 v12, v12\n\t"
    "v_exp_f32 v13, v13\n\t"
    "v_exp_f32 v14, v14\n\t"
    "v_exp_f32 v15, v15\n\t"
    "v_cvt_pk_bf16_f32 v20, v8, v9\n\t"
    "v_cvt_pk_bf16_f32 v21, v10, v11\n\t"
    "v_cvt_pk_bf16_f32 v22, v12, v13\n\t"
    "v_cvt_pk_bf16_f32 v23, v14, v15\n\t"
    "s_nop 1\n\t"
    "v_mfma_f32_16x16x32_bf16 v[96:99], a[128:131], v[20:23], 0\n\t"
    "v_sub_f32 v28, 0, v126\n\t"
    "v_sub_f32 v29, 0, v126\n\t"
    "v_mfma_f32_16x16x32_bf16 v[100:103], a[132:135], v[20:23], 0\n\t"
    "v_sub_f32 v30, 0, v126\n\t"
    "v_sub_f32 v31, 0, v126\n\t"
    "v_mfma_f32_16x16x32_bf16 v[104:107], a[136:139], v[20:23], 0\n\t"
    "v_mfma_f32_16x16x32_bf16 v[108:111], a[140:143], v[20:23], 0\n\t"
    "v_mfma_f32_16x16x32_bf16 v[112:115], a[144:147], v[20:23], 0\n\t"
    "v_mfma_f32_16x16x32_bf16 v[116:119], a[148:151], v[20:23], 0\n\t"
    "v_mfma_f32_16x16x32_bf16 v[120:123], a[152:155], v[20:23], 0\n\t"
    "v_mfma_f32_16x16x32_bf16 v[124:127], a[156:159], v[20:23], 0\n\t"
    "v_mfma_f32_16x16x32_bf16 a[164:167], a[184:187], v[20:23], 0\n\t"
    "s_sub_u32 s59, s57, %[lds]\n\t"
    "v_add_u32 v61, s59, v48\n\t"
    "v_add_u32 v62, s59, v49\n\t"
    "s_waitcnt vmcnt(13)\n\t"
    "ds_read_b128 v[128:131], v61 offset:0\n\t"
    "ds_read_b128 v[132:135], v62 offset:0\n\t"
    "s_add_u32 m0, s56, 5120\n\t"
    "s_add_u32 s58, s55, 20608\n\t"
    "buffer_load_dwordx4 v51, s[36:39], s58 offen nt lds\n\t"
    "buffer_load_dwordx4 v52, s[36:39], s58 offen offset:2048 nt lds\n\t"
    "s_waitcnt lgkmcnt(1)\n\t"
    "v_lshlrev_b32 v136, 16, v128\n\t"
    "v_and_b32 v137, s74, v128\n\t"
    "v_mul_f32 v136, s73, v136\n\t"
    "v_mul_f32 v137, s73, v137\n\t"
    "v_cvt_pk_bf16_f32 v136, v136, v137\n\t"
    "v_accvgpr_write_b32 a32, v136\n\t"
    "v_lshlrev_b32 v138, 16, v129\n\t"
    "v_and_b32 v139, s74, v129\n\t"
    "v_mul_f32 v138, s73, v138\n\t"
    "v_mul_f32 v139, s73, v139\n\t"
    "v_cvt_pk_bf16_f32 v138, v138, v139\n\t"
    "v_accvgpr_write_b32 a33, v138\n\t"
    "v_lshlrev_b32 v140, 16, v130\n\t"
    "v_and_b32 v141, s74, v130\n\t"
    "v_mul_f32 v140, s73, v140\n\t"
    "v_mul_f32 v141, s73, v141\n\t"
    "v_cvt_pk_bf16_f32 v140, v140, v141\n\t"
    "v_accvgpr_write_b32 a34, v140\n\t"
    "v_lshlrev_b32 v142, 16, v131\n\t"
    "v_and_b32 v143, s74, v131\n\t"
    "v_mul_f32 v142, s73, v142\n\t"
    "v_mul_f32 v143, s73, v143\n\t"
    "v_cvt_pk_bf16_f32 v142, v142, v143\n\t"
    "v_accvgpr_write_b32 a35, v142\n\t"
    "s_waitcnt lgkmcnt(0)\n\t"
    "v_lshlrev_b32 v144, 16, v132\n\t"
    "v_and_b32 v145, s74, v132\n\t"
    "v_mul_f32 v144, s73, v144\n\t"
    "v_mul_f32 v145, s73, v145\n\t"
    "v_cvt_pk_bf16_f32 v144, v144, v145\n\t"
    "v_accvgpr_write_b32 a36, v144\n\t"
    "v_lshlrev_b32 v146, 16, v133\n\t"
    "v_and_b32 v147, s74, v133\n\t"
    "v_mul_f32 v146, s73, v146\n\t"
    "v_mul_f32 v147, s73, v147\n\t"
    "v_cvt_pk_bf16_f32 v146, v146, v147\n\t"
    "v_accvgpr_write_b32 a37, v146\n\t"
    "v_lshlrev_b32 v148, 16, v134\n\t"
    "v_and_b32 v149, s74, v134\n\t"
    "v_mul_f32 v148, s73, v148\n\t"
    "v_mul_f32 v149, s73, v149\n\t"
    "v_cvt_pk_bf16_f32 v148, v148, v149\n\t"
    "v_accvgpr_write_b32 a38, v148\n\t"
    "v_lshlrev_b32 v150, 16, v135\n\t"
    "v_and_b32 v151, s74, v135\n\t"
    "v_mul_f32 v150, s73, v150\n\t"
    "v_mul_f32 v151, s73, v151\n\t"
    "v_cvt_pk_bf16_f32 v150, v150, v151\n\t"
    "v_accvgpr_write_b32 a39, v150\n\t"
    "s_waitcnt vmcnt(13)\n\t"
    "ds_read_b128 v[152:155], v61 offset:1024\n\t"
    "ds_read_b128 v[156:159], v62 offset:1024\n\t"
    "s_add_u32 m0, s53, 8192\n\t"
    "s_add_u32 s58, s52, 8192\n\t"
    "buffer_load_dwordx4 v51, s[40:43], s58 offen nt lds\n\t"
    "buffer_load_dwordx4 v52, s[40:43], s58 offen offset:2048 nt lds\n\t"
    "s_waitcnt lgkmcnt(1)\n\t"
    "v_lshlrev_b32 v136, 16, v152\n\t"
    "v_and_b32 v137, s74, v152\n\t"
    "v_mul_f32 v136, s73, v136\n\t"
    "v_mul_f32 v137, s73, v137\n\t"
    "v_cvt_pk_bf16_f32 v136, v136, v137\n\t"
    "v_accvgpr_write_b32 a40, v136\n\t"
    "v_lshlrev_b32 v138, 16, v153\n\t"
    "v_and_b32 v139, s74, v153\n\t"
    "v_mul_f32 v138, s73, v138\n\t"
    "v_mul_f32 v139, s73, v139\n\t"
    "v_cvt_pk_bf16_f32 v138, v138, v139\n\t"
    "v_accvgpr_write_b32 a41, v138\n\t"
    "v_lshlrev_b32 v140, 16, v154\n\t"
    "v_and_b32 v141, s74, v154\n\t"
    "v_mul_f32 v140, s73, v140\n\t"
    "v_mul_f32 v141, s73, v141\n\t"
    "v_cvt_pk_bf16_f32 v140, v140, v141\n\t"
    "v_accvgpr_write_b32 a42, v140\n\t"
    "v_lshlrev_b32 v142, 16, v155\n\t"
    "v_and_b32 v143, s74, v155\n\t"
    "v_mul_f32 v142, s73, v142\n\t"
    "v_mul_f32 v143, s73, v143\n\t"
    "v_cvt_pk_bf16_f32 v142, v142, v143\n\t"
    "v_accvgpr_write_b32 a43, v142\n\t"
    "s_waitcnt lgkmcnt(0)\n\t"
    "v_lshlrev_b32 v144, 16, v156\n\t"
    "v_and_b32 v145, s74, v156\n\t"
    "v_mul_f32 v144, s73, v144\n\t"
    "v_mul_f32 v145, s73, v145\n\t"
    "v_cvt_pk_bf16_f32 v144, v144, v145\n\t"
    "v_accvgpr_write_b32 a44, v144\n\t"
    "v_lshlrev_b32 v146, 16, v157\n\t"
    "v_and_b32 v147, s74, v157\n\t"
    "v_mul_f32 v146, s73, v146\n\t"
    "v_mul_f32 v147, s73, v147\n\t"
    "v_cvt_pk_bf16_f32 v146, v146, v147\n\t"
    "v_accvgpr_write_b32 a45, v146\n\t"
    "v_lshlrev_b32 v148, 16, v158\n\t"
    "v_and_b32 v149, s74, v158\n\t"
    "v_mul_f32 v148, s73, v148\n\t"
    "v_mul_f32 v149, s73, v149\n\t"
    "v_cvt_pk_bf16_f32 v148, v148, v149\n\t"
    "v_accvgpr_write_b32 a46, v148\n\t"
    "v_lshlrev_b32 v150, 16, v159\n\t"
    "v_and_b32 v151, s74, v159\n\t"
    "v_mul_f32 v150, s73, v150\n\t"
    "v_mul_f32 v151, s73, v151\n\t"
    "v_cvt_pk_bf16_f32 v150, v150, v151\n\t"
    "v_accvgpr_write_b32 a47, v150\n\t"
    "s_waitcnt vmcnt(12)\n\t"
    "ds_read_b128 v[160:163], v61 offset:4096\n\t"
    "ds_read_b128 v[164:167], v62 offset:4096\n\t"
    "s_add_u32 m0, s54, 8192\n\t"
    "s_nop 0\n\t"
    "buffer_load_dwordx4 v53, s[44:47], s58 offen nt lds\n\t"
    "buffer_load_dwordx4 v53, s[44:47], s58 offen offset:2048 nt lds\n\t"
    "s_waitcnt lgkmcnt(1)\n\t"
    "v_lshlrev_b32 v168, 16, v160\n\t"
    "v_and_b32 v169, s74, v160\n\t"
    "v_mul_f32 v168, s73, v168\n\t"
    "v_mul_f32 v169, s73, v169\n\t"
    "v_cvt_pk_bf16_f32 v168, v168, v169\n\t"
    "v_accvgpr_write_b32 a48, v168\n\t"
    "v_lshlrev_b32 v170, 16, v161\n\t"
    "v_and_b32 v171, s74, v161\n\t"
    "v_mul_f32 v170, s73, v170\n\t"
    "v_mul_f32 v171, s73, v171\n\t"
    "v_cvt_pk_bf16_f32 v170, v170, v171\n\t"
    "v_accvgpr_write_b32 a49, v170\n\t"
    "v_lshlrev_b32 v172, 16, v162\n\t"
    "v_and_b32 v173, s74, v162\n\t"
    "v_mul_f32 v172, s73, v172\n\t"
    "v_mul_f32 v173, s73, v173\n\t"
    "v_cvt_pk_bf16_f32 v172, v172, v173\n\t"
    "v_accvgpr_write_b32 a50, v172\n\t"
    "v_lshlrev_b32 v174, 16, v163\n\t"
    "v_and_b32 v175, s74, v163\n\t"
    "v_mul_f32 v174, s73, v174\n\t"
    "v_mul_f32 v175, s73, v175\n\t"
    "v_cvt_pk_bf16_f32 v174, v174, v175\n\t"
    "v_accvgpr_write_b32 a51, v174\n\t"
    "s_waitcnt lgkmcnt(0)\n\t"
    "v_lshlrev_b32 v176, 16, v164\n\t"
    "v_and_b32 v177, s74, v164\n\t"
    "v_mul_f32 v176, s73, v176\n\t"
    "v_mul_f32 v177, s73, v177\n\t"
    "v_cvt_pk_bf16_f32 v176, v176, v177\n\t"
    "v_accvgpr_write_b32 a52, v176\n\t"
    "v_lshlrev_b32 v178, 16, v165\n\t"
    "v_and_b32 v179, s74, v165\n\t"
    "v_mul_f32 v178, s73, v178\n\t"
    "v_mul_f32 v179, s73, v179\n\t"
    "v_cvt_pk_bf16_f32 v178, v178, v179\n\t"
    "v_accvgpr_write_b32 a53, v178\n\t"
    "v_lshlrev_b32 v180, 16, v166\n\t"
    "v_and_b32 v181, s74, v166\n\t"
    "v_mul_f32 v180, s73, v180\n\t"
    "v_mul_f32 v181, s73, v181\n\t"
    "v_cvt_pk_bf16_f32 v180, v180, v181\n\t"
    "v_accvgpr_write_b32 a54, v180\n\t"
    "v_lshlrev_b32 v182, 16, v167\n\t"
    "v_and_b32 v183, s74, v167\n\t"
    "v_mul_f32 v182, s73, v182\n\t"
    "v_mul_f32 v183, s73, v183\n\t"
    "v_cvt_pk_bf16_f32 v182, v182, v183\n\t"
    "v_accvgpr_write_b32 a55, v182\n\t"
    "s_waitcnt vmcnt(12)\n\t"
    "ds_read_b128 v[184:187], v61 offset:5120\n\t"
    "ds_read_b128 v[188:191], v62 offset:5120\n\t"
    "s_add_u32 m0, s53, 16384\n\t"
    "s_add_u32 s58, s52, 16384\n\t"
    "buffer_load_dwordx4 v51, s[40:43], s58 offen nt lds\n\t"
    "buffer_load_dwordx4 v52, s[40:43], s58 offen offset:2048 nt lds\n\t"
    "s_waitcnt lgkmcnt(1)\n\t"
    "v_lshlrev_b32 v168, 16, v184\n\t"
    "v_and_b32 v169, s74, v184\n\t"
    "v_mul_f32 v168, s73, v168\n\t"
    "v_mul_f32 v169, s73, v169\n\t"
    "v_cvt_pk_bf16_f32 v168, v168, v169\n\t"
    "v_accvgpr_write_b32 a56, v168\n\t"
    "v_lshlrev_b32 v170, 16, v185\n\t"
    "v_and_b32 v171, s74, v185\n\t"
    "v_mul_f32 v170, s73, v170\n\t"
    "v_mul_f32 v171, s73, v171\n\t"
    "v_cvt_pk_bf16_f32 v170, v170, v171\n\t"
    "v_accvgpr_write_b32 a57, v170\n\t"
    "v_lshlrev_b32 v172, 16, v186\n\t"
    "v_and_b32 v173, s74, v186\n\t"
    "v_mul_f32 v172, s73, v172\n\t"
    "v_mul_f32 v173, s73, v173\n\t"
    "v_cvt_pk_bf16_f32 v172, v172, v173\n\t"
    "v_accvgpr_write_b32 a58, v172\n\t"
    "v_lshlrev_b32 v174, 16, v187\n\t"
    "v_and_b32 v175, s74, v187\n\t"
    "v_mul_f32 v174, s73, v174\n\t"
    "v_mul_f32 v175, s73, v175\n\t"
    "v_cvt_pk_bf16_f32 v174, v174, v175\n\t"
    "v_accvgpr_write_b32 a59, v174\n\t"
    "s_waitcnt lgkmcnt(0)\n\t"
    "v_lshlrev_b32 v176, 16, v188\n\t"
    "v_and_b32 v177, s74, v188\n\t"
    "v_mul_f32 v176, s73, v176\n\t"
    "v_mul_f32 v177, s73, v177\n\t"
    "v_cvt_pk_bf16_f32 v176, v176, v177\n\t"
    "v_accvgpr_write_b32 a60, v176\n\t"
    "v_lshlrev_b32 v178, 16, v189\n\t"
    "v_and_b32 v179, s74, v189\n\t"
    "v_mul_f32 v178, s73, v178\n\t"
    "v_mul_f32 v179, s73, v179\n\t"
    "v_cvt_pk_bf16_f32 v178, v178, v179\n\t"
    "v_accvgpr_write_b32 a61, v178\n\t"
    "v_lshlrev_b32 v180, 16, v190\n\t"
    "v_and_b32 v181, s74, v190\n\t"
    "v_mul_f32 v180, s73, v180\n\t"
    "v_mul_f32 v181, s73, v181\n\t"
    "v_cvt_pk_bf16_f32 v180, v180, v181\n\t"
    "v_accvgpr_write_b32 a62, v180\n\t"
    "v_lshlrev_b32 v182, 16, v191\n\t"
    "v_and_b32 v183, s74, v191\n\t"
    "v_mul_f32 v182, s73, v182\n\t"
    "v_mul_f32 v183, s73, v183\n\t"
    "v_cvt_pk_bf16_f32 v182, v182, v183\n\t"
    "v_accvgpr_write_b32 a63, v182\n\t"
    "v_mfma_f32_16x16x32_bf16 v[0:3], a[96:99], a[32:35], 0\n\t"
    "v_mfma_f32_16x16x32_bf16 v[0:3], a[100:103], a[36:39], v[0:3]\n\t"
    "v_mfma_f32_16x16x32_bf16 v[0:3], a[104:107], a[40:43], v[0:3]\n\t"
    "v_mfma_f32_16x16x32_bf16 v[0:3], a[108:111], a[44:47], v[0:3]\n\t"
    "v_mfma_f32_16x16x32_bf16 v[4:7], a[112:115], a[32:35], 0\n\t"
    "v_mfma_f32_16x16x32_bf16 v[4:7], a[116:119], a[36:39], v[4:7]\n\t"
    "v_mfma_f32_16x16x32_bf16 v[4:7], a[120:123], a[40:43], v[4:7]\n\t"
    "v_mfma_f32_16x16x32_bf16 v[4:7], a[124:127], a[44:47], v[4:7]\n\t"
    "v_mfma_f32_16x16x32_bf16 v[8:11], a[96:99], a[48:51], 0\n\t"
    "v_mfma_f32_16x16x32_bf16 v[8:11], a[100:103], a[52:55], v[8:11]\n\t"
    "v_mfma_f32_16x16x32_bf16 v[8:11], a[104:107], a[56:59], v[8:11]\n\t"
    "v_mfma_f32_16x16x32_bf16 v[8:11], a[108:111], a[60:63], v[8:11]\n\t"
    "v_max3_f32 v158, v0, v1, v2\n\t"
    "v_mfma_f32_16x16x32_bf16 v[12:15], a[112:115], a[48:51], 0\n\t"
    "v_mfma_f32_16x16x32_bf16 v[12:15], a[116:119], a[52:55], v[12:15]\n\t"
    "v_mfma_f32_16x16x32_bf16 v[12:15], a[120:123], a[56:59], v[12:15]\n\t"
    "v_max3_f32 v159, v3, v4, v5\n\t"
    "v_max3_f32 v158, v158, v6, v7\n\t"
    "v_max_f32 v158, v158, v159\n\t"
    "v_mov_b32 v159, v158\n\t"
    "v_mfma_f32_16x16x32_bf16 v[12:15], a[124:127], a[60:63], v[12:15]\n\t"
    "v_max3_f32 v190, v8, v9, v10\n\t"
    "v_permlane32_swap_b32 v158, v159\n\t"
    "v_max_f32 v158, v158, v159\n\t"
    "v_mov_b32 v159, v158\n\t"
    "s_nop 1\n\t"
    "v_permlane16_swap_b32 v158, v159\n\t"
    "v_max_f32 v158, v158, v159\n\t"
    "v_sub_f32 v0, v0, v158\n\t"
    "v_sub_f32 v1, v1, v158\n\t"
    "v_sub_f32 v2, v2, v158\n\t"
    "v_sub_f32 v3, v3, v158\n\t"
    "v_sub_f32 v4, v4, v158\n\t"
    "v_sub_f32 v5, v5, v158\n\t"
    "v_sub_f32 v6, v6, v158\n\t"
    "v_sub_f32 v7, v7, v158\n\t"
    "v_exp_f32 v0, v0\n\t"
    "v_exp_f32 v1, v1\n\t"
    "v_exp_f32 v2, v2\n\t"
    "v_exp_f32 v3, v3\n\t"
    "v_exp_f32 v4, v4\n\t"
    "v_exp_f32 v5, v5\n\t"
    "v_exp_f32 v6, v6\n\t"
    "v_exp_f32 v7, v7\n\t"
    "v_cvt_pk_bf16_f32 v16, v0, v1\n\t"
    "v_cvt_pk_bf16_f32 v17, v2, v3\n\t"
    "v_cvt_pk_bf16_f32 v18, v4, v5\n\t"
    "v_cvt_pk_bf16_f32 v19, v6, v7\n\t"
    "s_nop 1\n\t"
    "v_mfma_f32_16x16x32_bf16 v[128:131], a[128:131], v[16:19], 0\n\t"
    "v_max3_f32 v191, v11, v12, v13\n\t"
    "v_max3_f32 v190, v190, v14, v15\n\t"
    "v_mfma_f32_16x16x32_bf16 v[132:135], a[132:135], v[16:19], 0\n\t"
    "v_max_f32 v190, v190, v191\n\t"
    "v_mov_b32 v191, v190\n\t"
    "v_mfma_f32_16x16x32_bf16 v[136:139], a[136:139], v[16:19], 0\n\t"
    "v_sub_f32 v32, 0, v158\n\t"
    "v_sub_f32 v33, 0, v158\n\t"
    "v_mfma_f32_16x16x32_bf16 v[140:143], a[140:143], v[16:19], 0\n\t"
    "v_sub_f32 v34, 0, v158\n\t"
    "v_sub_f32 v35, 0, v158\n\t"
    "v_mfma_f32_16x16x32_bf16 v[144:147], a[144:147], v[16:19], 0\n\t"
    "v_permlane32_swap_b32 v190, v191\n\t"
    "v_max_f32 v190, v190, v191\n\t"
    "v_mfma_f32_16x16x32_bf16 v[148:151], a[148:151], v[16:19], 0\n\t"
    "v_mov_b32 v191, v190\n\t"
    "v_mfma_f32_16x16x32_bf16 v[152:155], a[152:155], v[16:19], 0\n\t"
    "v_mfma_f32_16x16x32_bf16 v[156:159], a[156:159], v[16:19], 0\n\t"
    "v_permlane16_swap_b32 v190, v191\n\t"
    "v_max_f32 v190, v190, v191\n\t"
    "v_sub_f32 v8, v8, v190\n\t"
    "v_sub_f32 v9, v9, v190\n\t"
    "v_sub_f32 v10, v10, v190\n\t"
    "v_sub_f32 v11, v11, v190\n\t"
    "v_mfma_f32_16x16x32_bf16 a[168:171], a[184:187], v[16:19], 0\n\t"
    "v_sub_f32 v12, v12, v190\n\t"
    "v_sub_f32 v13, v13, v190\n\t"
    "v_sub_f32 v14, v14, v190\n\t"
    "v_sub_f32 v15, v15, v190\n\t"
    "v_exp_f32 v8, v8\n\t"
    "v_exp_f32 v9, v9\n\t"
    "v_exp_f32 v10, v10\n\t"
    "v_exp_f32 v11, v11\n\t"
    "v_exp_f32 v12, v12\n\t"
    "v_exp_f32 v13, v13\n\t"
    "v_exp_f32 v14, v14\n\t"
    "v_exp_f32 v15, v15\n\t"
    "v_cvt_pk_bf16_f32 v20, v8, v9\n\t"
    "v_cvt_pk_bf16_f32 v21, v10, v11\n\t"
    "v_cvt_pk_bf16_f32 v22, v12, v13\n\t"
    "v_cvt_pk_bf16_f32 v23, v14, v15\n\t"
    "s_nop 1\n\t"
    "v_mfma_f32_16x16x32_bf16 v[160:163], a[128:131], v[20:23], 0\n\t"
    "v_sub_f32 v36, 0, v190\n\t"
    "v_sub_f32 v37, 0, v190\n\t"
    "v_mfma_f32_16x16x32_bf16 v[164:167], a[132:135], v[20:23], 0\n\t"
    "v_sub_f32 v38, 0, v190\n\t"
    "v_sub_f32 v39, 0, v190\n\t"
    "v_mfma_f32_16x16x32_bf16 v[168:171], a[136:139], v[20:23], 0\n\t"
    "v_mfma_f32_16x16x32_bf16 v[172:175], a[140:143], v[20:23], 0\n\t"
    "v_mfma_f32_16x16x32_bf16 v[176:179], a[144:147], v[20:23], 0\n\t"
    "v_mfma_f32_16x16x32_bf16 v[180:183], a[148:151], v[20:23], 0\n\t"
    "v_mfma_f32_16x16x32_bf16 v[184:187], a[152:155], v[20:23], 0\n\t"
    "v_mfma_f32_16x16x32_bf16 v[188:191], a[156:159], v[20:23], 0\n\t"
    "v_mfma_f32_16x16x32_bf16 a[172:175], a[184:187], v[20:23], 0\n\t"
    "s_sub_u32 s59, s56, %[lds]\n\t"
    "v_add_u32 v61, s59, v48\n\t"
    "v_add_u32 v62, s59, v49\n\t"
    "s_waitcnt vmcnt(12)\n\t"
    "ds_read_b128 v[192:195], v61 offset:0\n\t"
    "ds_read_b128 v[196:199], v62 offset:0\n\t"
    "s_add_u32 m0, s54, 16384\n\t"
    "s_nop 0\n\t"
    "buffer_load_dwordx4 v53, s[44:47], s58 offen nt lds\n\t"
    "buffer_load_dwordx4 v53, s[44:47], s58 offen offset:2048 nt lds\n\t"
    "s_waitcnt lgkmcnt(1)\n\t"
    "v_lshlrev_b32 v200, 16, v192\n\t"
    "v_and_b32 v201, s74, v192\n\t"
    "v_mul_f32 v200, s73, v200\n\t"
    "v_mul_f32 v201, s73, v201\n\t"
    "v_cvt_pk_bf16_f32 v200, v200, v201\n\t"
    "v_accvgpr_write_b32 a64, v200\n\t"
    "v_lshlrev_b32 v202, 16, v193\n\t"
    "v_and_b32 v203, s74, v193\n\t"
    "v_mul_f32 v202, s73, v202\n\t"
    "v_mul_f32 v203, s73, v203\n\t"
    "v_cvt_pk_bf16_f32 v202, v202, v203\n\t"
    "v_accvgpr_write_b32 a65, v202\n\t"
    "v_lshlrev_b32 v204, 16, v194\n\t"
    "v_and_b32 v205, s74, v194\n\t"
    "v_mul_f32 v204, s73, v204\n\t"
    "v_mul_f32 v205, s73, v205\n\t"
    "v_cvt_pk_bf16_f32 v204, v204, v205\n\t"
    "v_accvgpr_write_b32 a66, v204\n\t"
    "v_lshlrev_b32 v206, 16, v195\n\t"
    "v_and_b32 v207, s74, v195\n\t"
    "v_mul_f32 v206, s73, v206\n\t"
    "v_mul_f32 v207, s73, v207\n\t"
    "v_cvt_pk_bf16_f32 v206, v206, v207\n\t"
    "v_accvgpr_write_b32 a67, v206\n\t"
    "s_waitcnt lgkmcnt(0)\n\t"
    "v_lshlrev_b32 v208, 16, v196\n\t"
    "v_and_b32 v209, s74, v196\n\t"
    "v_mul_f32 v208, s73, v208\n\t"
    "v_mul_f32 v209, s73, v209\n\t"
    "v_cvt_pk_bf16_f32 v208, v208, v209\n\t"
    "v_accvgpr_write_b32 a68, v208\n\t"
    "v_lshlrev_b32 v210, 16, v197\n\t"
    "v_and_b32 v211, s74, v197\n\t"
    "v_mul_f32 v210, s73, v210\n\t"
    "v_mul_f32 v211, s73, v211\n\t"
    "v_cvt_pk_bf16_f32 v210, v210, v211\n\t"
    "v_accvgpr_write_b32 a69, v210\n\t"
    "v_lshlrev_b32 v212, 16, v198\n\t"
    "v_and_b32 v213, s74, v198\n\t"
    "v_mul_f32 v212, s73, v212\n\t"
    "v_mul_f32 v213, s73, v213\n\t"
    "v_cvt_pk_bf16_f32 v212, v212, v213\n\t"
    "v_accvgpr_write_b32 a70, v212\n\t"
    "v_lshlrev_b32 v214, 16, v199\n\t"
    "v_and_b32 v215, s74, v199\n\t"
    "v_mul_f32 v214, s73, v214\n\t"
    "v_mul_f32 v215, s73, v215\n\t"
    "v_cvt_pk_bf16_f32 v214, v214, v215\n\t"
    "v_accvgpr_write_b32 a71, v214\n\t"
    "s_waitcnt vmcnt(12)\n\t"
    "ds_read_b128 v[216:219], v61 offset:1024\n\t"
    "ds_read_b128 v[220:223], v62 offset:1024\n\t"
    "s_add_u32 m0, s53, 24576\n\t"
    "s_add_u32 s58, s52, 24576\n\t"
    "buffer_load_dwordx4 v51, s[40:43], s58 offen nt lds\n\t"
    "buffer_load_dwordx4 v52, s[40:43], s58 offen offset:2048 nt lds\n\t"
    "s_waitcnt lgkmcnt(1)\n\t"
    "v_lshlrev_b32 v200, 16, v216\n\t"
    "v_and_b32 v201, s74, v216\n\t"
    "v_mul_f32 v200, s73, v200\n\t"
    "v_mul_f32 v201, s73, v201\n\t"
    "v_cvt_pk_bf16_f32 v200, v200, v201\n\t"
    "v_accvgpr_write_b32 a72, v200\n\t"
    "v_lshlrev_b32 v202, 16, v217\n\t"
    "v_and_b32 v203, s74, v217\n\t"
    "v_mul_f32 v202, s73, v202\n\t"
    "v_mul_f32 v203, s73, v203\n\t"
    "v_cvt_pk_bf16_f32 v202, v202, v203\n\t"
    "v_accvgpr_write_b32 a73, v202\n\t"
    "v_lshlrev_b32 v204, 16, v218\n\t"
    "v_and_b32 v205, s74, v218\n\t"
    "v_mul_f32 v204, s73, v204\n\t"
    "v_mul_f32 v205, s73, v205\n\t"
    "v_cvt_pk_bf16_f32 v204, v204, v205\n\t"
    "v_accvgpr_write_b32 a74, v204\n\t"
    "v_lshlrev_b32 v206, 16, v219\n\t"
    "v_and_b32 v207, s74, v219\n\t"
    "v_mul_f32 v206, s73, v206\n\t"
    "v_mul_f32 v207, s73, v207\n\t"
    "v_cvt_pk_bf16_f32 v206, v206, v207\n\t"
    "v_accvgpr_write_b32 a75, v206\n\t"
    "s_waitcnt lgkmcnt(0)\n\t"
    "v_lshlrev_b32 v208, 16, v220\n\t"
    "v_and_b32 v209, s74, v220\n\t"
    "v_mul_f32 v208, s73, v208\n\t"
    "v_mul_f32 v209, s73, v209\n\t"
    "v_cvt_pk_bf16_f32 v208, v208, v209\n\t"
    "v_accvgpr_write_b32 a76, v208\n\t"
    "v_lshlrev_b32 v210, 16, v221\n\t"
    "v_and_b32 v211, s74, v221\n\t"
    "v_mul_f32 v210, s73, v210\n\t"
    "v_mul_f32 v211, s73, v211\n\t"
    "v_cvt_pk_bf16_f32 v210, v210, v211\n\t"
    "v_accvgpr_write_b32 a77, v210\n\t"
    "v_lshlrev_b32 v212, 16, v222\n\t"
    "v_and_b32 v213, s74, v222\n\t"
    "v_mul_f32 v212, s73, v212\n\t"
    "v_mul_f32 v213, s73, v213\n\t"
    "v_cvt_pk_bf16_f32 v212, v212, v213\n\t"
    "v_accvgpr_write_b32 a78, v212\n\t"
    "v_lshlrev_b32 v214, 16, v223\n\t"
    "v_and_b32 v215, s74, v223\n\t"
    "v_mul_f32 v214, s73, v214\n\t"
    "v_mul_f32 v215, s73, v215\n\t"
    "v_cvt_pk_bf16_f32 v214, v214, v215\n\t"
    "v_accvgpr_write_b32 a79, v214\n\t"
    "s_waitcnt vmcnt(12)\n\t"
    "ds_read_b128 v[224:227], v61 offset:4096\n\t"
    "ds_read_b128 v[228:231], v62 offset:4096\n\t"
    "s_add_u32 m0, s54, 24576\n\t"
    "s_nop 0\n\t"
    "buffer_load_dwordx4 v53, s[44:47], s58 offen nt lds\n\t"
    "buffer_load_dwordx4 v53, s[44:47], s58 offen offset:2048 nt lds\n\t"
    "s_waitcnt lgkmcnt(1)\n\t"
    "v_lshlrev_b32 v232, 16, v224\n\t"
    "v_and_b32 v233, s74, v224\n\t"
    "v_mul_f32 v232, s73, v232\n\t"
    "v_mul_f32 v233, s73, v233\n\t"
    "v_cvt_pk_bf16_f32 v232, v232, v233\n\t"
    "v_accvgpr_write_b32 a80, v232\n\t"
    "v_lshlrev_b32 v234, 16, v225\n\t"
    "v_and_b32 v235, s74, v225\n\t"
    "v_mul_f32 v234, s73, v234\n\t"
    "v_mul_f32 v235, s73, v235\n\t"
    "v_cvt_pk_bf16_f32 v234, v234, v235\n\t"
    "v_accvgpr_write_b32 a81, v234\n\t"
    "v_lshlrev_b32 v236, 16, v226\n\t"
    "v_and_b32 v237, s74, v226\n\t"
    "v_mul_f32 v236, s73, v236\n\t"
    "v_mul_f32 v237, s73, v237\n\t"
    "v_cvt_pk_bf16_f32 v236, v236, v237\n\t"
    "v_accvgpr_write_b32 a82, v236\n\t"
    "v_lshlrev_b32 v238, 16, v227\n\t"
    "v_and_b32 v239, s74, v227\n\t"
    "v_mul_f32 v238, s73, v238\n\t"
    "v_mul_f32 v239, s73, v239\n\t"
    "v_cvt_pk_bf16_f32 v238, v238, v239\n\t"
    "v_accvgpr_write_b32 a83, v238\n\t"
    "s_waitcnt lgkmcnt(0)\n\t"
    "v_lshlrev_b32 v240, 16, v228\n\t"
    "v_and_b32 v241, s74, v228\n\t"
    "v_mul_f32 v240, s73, v240\n\t"
    "v_mul_f32 v241, s73, v241\n\t"
    "v_cvt_pk_bf16_f32 v240, v240, v241\n\t"
    "v_accvgpr_write_b32 a84, v240\n\t"
    "v_lshlrev_b32 v242, 16, v229\n\t"
    "v_and_b32 v243, s74, v229\n\t"
    "v_mul_f32 v242, s73, v242\n\t"
    "v_mul_f32 v243, s73, v243\n\t"
    "v_cvt_pk_bf16_f32 v242, v242, v243\n\t"
    "v_accvgpr_write_b32 a85, v242\n\t"
    "v_lshlrev_b32 v244, 16, v230\n\t"
    "v_and_b32 v245, s74, v230\n\t"
    "v_mul_f32 v244, s73, v244\n\t"
    "v_mul_f32 v245, s73, v245\n\t"
    "v_cvt_pk_bf16_f32 v244, v244, v245\n\t"
    "v_accvgpr_write_b32 a86, v244\n\t"
    "v_lshlrev_b32 v246, 16, v231\n\t"
    "v_and_b32 v247, s74, v231\n\t"
    "v_mul_f32 v246, s73, v246\n\t"
    "v_mul_f32 v247, s73, v247\n\t"
    "v_cvt_pk_bf16_f32 v246, v246, v247\n\t"
    "v_accvgpr_write_b32 a87, v246\n\t"
    "s_waitcnt vmcnt(12)\n\t"
    "ds_read_b128 v[248:251], v61 offset:5120\n\t"
    "ds_read_b128 v[252:255], v62 offset:5120\n\t"
    "s_waitcnt lgkmcnt(1)\n\t"
    "v_lshlrev_b32 v232, 16, v248\n\t"
    "v_and_b32 v233, s74, v248\n\t"
    "v_mul_f32 v232, s73, v232\n\t"
    "v_mul_f32 v233, s73, v233\n\t"
    "v_cvt_pk_bf16_f32 v232, v232, v233\n\t"
    "v_accvgpr_write_b32 a88, v232\n\t"
    "v_lshlrev_b32 v234, 16, v249\n\t"
    "v_and_b32 v235, s74, v249\n\t"
    "v_mul_f32 v234, s73, v234\n\t"
    "v_mul_f32 v235, s73, v235\n\t"
    "v_cvt_pk_bf16_f32 v234, v234, v235\n\t"
    "v_accvgpr_write_b32 a89, v234\n\t"
    "v_lshlrev_b32 v236, 16, v250\n\t"
    "v_and_b32 v237, s74, v250\n\t"
    "v_mul_f32 v236, s73, v236\n\t"
    "v_mul_f32 v237, s73, v237\n\t"
    "v_cvt_pk_bf16_f32 v236, v236, v237\n\t"
    "v_accvgpr_write_b32 a90, v236\n\t"
    "v_lshlrev_b32 v238, 16, v251\n\t"
    "v_and_b32 v239, s74, v251\n\t"
    "v_mul_f32 v238, s73, v238\n\t"
    "v_mul_f32 v239, s73, v239\n\t"
    "v_cvt_pk_bf16_f32 v238, v238, v239\n\t"
    "v_accvgpr_write_b32 a91, v238\n\t"
    "s_waitcnt lgkmcnt(0)\n\t"
    "v_lshlrev_b32 v240, 16, v252\n\t"
    "v_and_b32 v241, s74, v252\n\t"
    "v_mul_f32 v240, s73, v240\n\t"
    "v_mul_f32 v241, s73, v241\n\t"
    "v_cvt_pk_bf16_f32 v240, v240, v241\n\t"
    "v_accvgpr_write_b32 a92, v240\n\t"
    "v_lshlrev_b32 v242, 16, v253\n\t"
    "v_and_b32 v243, s74, v253\n\t"
    "v_mul_f32 v242, s73, v242\n\t"
    "v_mul_f32 v243, s73, v243\n\t"
    "v_cvt_pk_bf16_f32 v242, v242, v243\n\t"
    "v_accvgpr_write_b32 a93, v242\n\t"
    "v_lshlrev_b32 v244, 16, v254\n\t"
    "v_and_b32 v245, s74, v254\n\t"
    "v_mul_f32 v244, s73, v244\n\t"
    "v_mul_f32 v245, s73, v245\n\t"
    "v_cvt_pk_bf16_f32 v244, v244, v245\n\t"
    "v_accvgpr_write_b32 a94, v244\n\t"
    "v_lshlrev_b32 v246, 16, v255\n\t"
    "v_and_b32 v247, s74, v255\n\t"
    "v_mul_f32 v246, s73, v246\n\t"
    "v_mul_f32 v247, s73, v247\n\t"
    "v_cvt_pk_bf16_f32 v246, v246, v247\n\t"
    "v_accvgpr_write_b32 a95, v246\n\t"
    "v_mfma_f32_16x16x32_bf16 v[0:3], a[96:99], a[64:67], 0\n\t"
    "v_mfma_f32_16x16x32_bf16 v[0:3], a[100:103], a[68:71], v[0:3]\n\t"
    "v_mfma_f32_16x16x32_bf16 v[0:3], a[104:107], a[72:75], v[0:3]\n\t"
    "v_mfma_f32_16x16x32_bf16 v[0:3], a[108:111], a[76:79], v[0:3]\n\t"
    "v_mfma_f32_16x16x32_bf16 v[4:7], a[112:115], a[64:67], 0\n\t"
    "v_mfma_f32_16x16x32_bf16 v[4:7], a[116:119], a[68:71], v[4:7]\n\t"
    "v_mfma_f32_16x16x32_bf16 v[4:7], a[120:123], a[72:75], v[4:7]\n\t"
    "v_mfma_f32_16x16x32_bf16 v[4:7], a[124:127], a[76:79], v[4:7]\n\t"
    "v_mfma_f32_16x16x32_bf16 v[8:11], a[96:99], a[80:83], 0\n\t"
    "v_mfma_f32_16x16x32_bf16 v[8:11], a[100:103], a[84:87], v[8:11]\n\t"
    "v_mfma_f32_16x16x32_bf16 v[8:11], a[104:107], a[88:91], v[8:11]\n\t"
    "v_mfma_f32_16x16x32_bf16 v[8:11], a[108:111], a[92:95], v[8:11]\n\t"
    "v_max3_f32 v222, v0, v1, v2\n\t"
    "v_mfma_f32_16x16x32_bf16 v[12:15], a[112:115], a[80:83], 0\n\t"
    "v_mfma_f32_16x16x32_bf16 v[12:15], a[116:119], a[84:87], v[12:15]\n\t"
    "v_mfma_f32_16x16x32_bf16 v[12:15], a[120:123], a[88:91], v[12:15]\n\t"
    "v_max3_f32 v223, v3, v4, v5\n\t"
    "v_max3_f32 v222, v222, v6, v7\n\t"
    "v_max_f32 v222, v222, v223\n\t"
    "v_mov_b32 v223, v222\n\t"
    "v_mfma_f32_16x16x32_bf16 v[12:15], a[124:127], a[92:95], v[12:15]\n\t"
    "v_max3_f32 v254, v8, v9, v10\n\t"
    "v_permlane32_swap_b32 v222, v223\n\t"
    "v_max_f32 v222, v222, v223\n\t"
    "v_mov_b32 v223, v222\n\t"
    "s_nop 1\n\t"
    "v_permlane16_swap_b32 v222, v223\n\t"
    "v_max_f32 v222, v222, v223\n\t"
    "v_sub_f32 v0, v0, v222\n\t"
    "v_sub_f32 v1, v1, v222\n\t"
    "v_sub_f32 v2, v2, v222\n\t"
    "v_sub_f32 v3, v3, v222\n\t"
    "v_sub_f32 v4, v4, v222\n\t"
    "v_sub_f32 v5, v5, v222\n\t"
    "v_sub_f32 v6, v6, v222\n\t"
    "v_sub_f32 v7, v7, v222\n\t"
    "v_exp_f32 v0, v0\n\t"
    "v_exp_f32 v1, v1\n\t"
    "v_exp_f32 v2, v2\n\t"
    "v_exp_f32 v3, v3\n\t"
    "v_exp_f32 v4, v4\n\t"
    "v_exp_f32 v5, v5\n\t"
    "v_exp_f32 v6, v6\n\t"
    "v_exp_f32 v7, v7\n\t"
    "v_cvt_pk_bf16_f32 v16, v0, v1\n\t"
    "v_cvt_pk_bf16_f32 v17, v2, v3\n\t"
    "v_cvt_pk_bf16_f32 v18, v4, v5\n\t"
    "v_cvt_pk_bf16_f32 v19, v6, v7\n\t"
    "s_nop 1\n\t"
    "v_mfma_f32_16x16x32_bf16 v[192:195], a[128:131], v[16:19], 0\n\t"
    "v_max3_f32 v255, v11, v12, v13\n\t"
    "v_max3_f32 v254, v254, v14, v15\n\t"
    "v_mfma_f32_16x16x32_bf16 v[196:199], a[132:135], v[16:19], 0\n\t"
    "v_max_f32 v254, v254, v255\n\t"
    "v_mov_b32 v255, v254\n\t"
    "v_mfma_f32_16x16x32_bf16 v[200:203], a[136:139], v[16:19], 0\n\t"
    "v_sub_f32 v40, 0, v222\n\t"
    "v_sub_f32 v41, 0, v222\n\t"
    "v_mfma_f32_16x16x32_bf16 v[204:207], a[140:143], v[16:19], 0\n\t"
    "v_sub_f32 v42, 0, v222\n\t"
    "v_sub_f32 v43, 0, v222\n\t"
    "v_mfma_f32_16x16x32_bf16 v[208:211], a[144:147], v[16:19], 0\n\t"
    "v_permlane32_swap_b32 v254, v255\n\t"
    "v_max_f32 v254, v254, v255\n\t"
    "v_mfma_f32_16x16x32_bf16 v[212:215], a[148:151], v[16:19], 0\n\t"
    "v_mov_b32 v255, v254\n\t"
    "v_mfma_f32_16x16x32_bf16 v[216:219], a[152:155], v[16:19], 0\n\t"
    "v_mfma_f32_16x16x32_bf16 v[220:223], a[156:159], v[16:19], 0\n\t"
    "v_permlane16_swap_b32 v254, v255\n\t"
    "v_max_f32 v254, v254, v255\n\t"
    "v_sub_f32 v8, v8, v254\n\t"
    "v_sub_f32 v9, v9, v254\n\t"
    "v_sub_f32 v10, v10, v254\n\t"
    "v_sub_f32 v11, v11, v254\n\t"
    "v_mfma_f32_16x16x32_bf16 a[176:179], a[184:187], v[16:19], 0\n\t"
    "v_sub_f32 v12, v12, v254\n\t"
    "v_sub_f32 v13, v13, v254\n\t"
    "v_sub_f32 v14, v14, v254\n\t"
    "v_sub_f32 v15, v15, v254\n\t"
    "v_exp_f32 v8, v8\n\t"
    "v_exp_f32 v9, v9\n\t"
    "v_exp_f32 v10, v10\n\t"
    "v_exp_f32 v11, v11\n\t"
    "v_exp_f32 v12, v12\n\t"
    "v_exp_f32 v13, v13\n\t"
    "v_exp_f32 v14, v14\n\t"
    "v_exp_f32 v15, v15\n\t"
    "v_cvt_pk_bf16_f32 v20, v8, v9\n\t"
    "v_cvt_pk_bf16_f32 v21, v10, v11\n\t"
    "v_cvt_pk_bf16_f32 v22, v12, v13\n\t"
    "v_cvt_pk_bf16_f32 v23, v14, v15\n\t"
    "s_nop 1\n\t"
    "v_mfma_f32_16x16x32_bf16 v[224:227], a[128:131], v[20:23], 0\n\t"
    "v_sub_f32 v44, 0, v254\n\t"
    "v_sub_f32 v45, 0, v254\n\t"
    "v_mfma_f32_16x16x32_bf16 v[228:231], a[132:135], v[20:23], 0\n\t"
    "v_sub_f32 v46, 0, v254\n\t"
    "v_sub_f32 v47, 0, v254\n\t"
    "v_mfma_f32_16x16x32_bf16 v[232:235], a[136:139], v[20:23], 0\n\t"
    "v_mfma_f32_16x16x32_bf16 v[236:239], a[140:143], v[20:23], 0\n\t"
    "v_mfma_f32_16x16x32_bf16 v[240:243], a[144:147], v[20:23], 0\n\t"
    "v_mfma_f32_16x16x32_bf16 v[244:247], a[148:151], v[20:23], 0\n\t"
    "v_mfma_f32_16x16x32_bf16 v[248:251], a[152:155], v[20:23], 0\n\t"
    "v_mfma_f32_16x16x32_bf16 v[252:255], a[156:159], v[20:23], 0\n\t"
    "v_mfma_f32_16x16x32_bf16 a[180:183], a[184:187], v[20:23], 0\n\t"
    "s_waitcnt vmcnt(8)\n\t"
    "s_barrier\n\t"
    "s_add_u32 m0, s53, 32768\n\t"
    "s_add_u32 s58, s52, 32768\n\t"
    "buffer_load_dwordx4 v51, s[40:43], s58 offen nt lds\n\t"
    "buffer_load_dwordx4 v52, s[40:43], s58 offen offset:2048 nt lds\n\t"
    "s_add_u32 m0, s54, 32768\n\t"
    "s_nop 0\n\t"
    "buffer_load_dwordx4 v53, s[44:47], s58 offen nt lds\n\t"
    "buffer_load_dwordx4 v53, s[44:47], s58 offen offset:2048 nt lds\n\t"
    "s_add_u32 m0, s53, 40960\n\t"
    "s_add_u32 s58, s52, 40960\n\t"
    "buffer_load_dwordx4 v51, s[40:43], s58 offen nt lds\n\t"
    "buffer_load_dwordx4 v52, s[40:43], s58 offen offset:2048 nt lds\n\t"
    "ds_read_b128 a[96:99], v48 offset:8192\n\t"
    "s_waitcnt lgkmcnt(0)\n\t"
    "v_mfma_f32_16x16x32_bf16 v[0:3], a[96:99], a[0:3], v[24:27]\n\t"
    "ds_read_b128 a[100:103], v49 offset:8192\n\t"
    "ds_read_b128 a[104:107], v48 offset:9216\n\t"
    "ds_read_b128 a[108:111], v49 offset:9216\n\t"
    "ds_read_b128 a[112:115], v48 offset:12288\n\t"
    "ds_read_b128 a[116:119], v49 offset:12288\n\t"
    "ds_read_b128 a[120:123], v48 offset:13312\n\t"
    "s_waitcnt lgkmcnt(5)\n\t"
    "v_mfma_f32_16x16x32_bf16 v[0:3], a[100:103], a[4:7], v[0:3]\n\t"
    "ds_read_b128 a[124:127], v49 offset:13312\n\t"
    "ds_read_b64_tr_b16 a[128:129], v50 offset:8192\n\t"
    "s_waitcnt lgkmcnt(6)\n\t"
    "v_mfma_f32_16x16x32_bf16 v[0:3], a[104:107], a[8:11], v[0:3]\n\t"
    "ds_read_b64_tr_b16 a[130:131], v50 offset:12288\n\t"
    "ds_read_b64_tr_b16 a[132:133], v50 offset:8448\n\t"
    "s_waitcnt lgkmcnt(7)\n\t"
    "v_mfma_f32_16x16x32_bf16 v[0:3], a[108:111], a[12:15], v[0:3]\n\t"
    "ds_read_b64_tr_b16 a[134:135], v50 offset:12544\n\t"
    "ds_read_b64_tr_b16 a[136:137], v50 offset:8704\n\t"
    "s_waitcnt lgkmcnt(8)\n\t"
    "v_mfma_f32_16x16x32_bf16 v[4:7], a[112:115], a[0:3], v[24:27]\n\t"
    "ds_read_b64_tr_b16 a[138:139], v50 offset:12800\n\t"
    "ds_read_b64_tr_b16 a[140:141], v50 offset:8960\n\t"
    "s_waitcnt lgkmcnt(9)\n\t"
    "v_mfma_f32_16x16x32_bf16 v[4:7], a[116:119], a[4:7], v[4:7]\n\t"
    "ds_read_b64_tr_b16 a[142:143], v50 offset:13056\n\t"
    "ds_read_b64_tr_b16 a[144:145], v50 offset:9216\n\t"
    "s_waitcnt lgkmcnt(10)\n\t"
    "v_mfma_f32_16x16x32_bf16 v[4:7], a[120:123], a[8:11], v[4:7]\n\t"
    "v_exp_f32 v0, v0\n\t"
    "s_waitcnt lgkmcnt(9)\n\t"
    "v_mfma_f32_16x16x32_bf16 v[4:7], a[124:127], a[12:15], v[4:7]\n\t"
    "v_exp_f32 v1, v1\n\t"
    "v_mfma_f32_16x16x32_bf16 v[8:11], a[96:99], a[16:19], v[28:31]\n\t"
    "v_exp_f32 v2, v2\n\t"
    "v_mfma_f32_16x16x32_bf16 v[8:11], a[100:103], a[20:23], v[8:11]\n\t"
    "v_exp_f32 v3, v3\n\t"
    "v_mfma_f32_16x16x32_bf16 v[8:11], a[104:107], a[24:27], v[8:11]\n\t"
    "ds_read_b64_tr_b16 a[146:147], v50 offset:13312\n\t"
    "v_cvt_pk_bf16_f32 v16, v0, v1\n\t"
    "v_mfma_f32_16x16x32_bf16 v[8:11], a[108:111], a[28:31], v[8:11]\n\t"
    "ds_read_b64_tr_b16 a[148:149], v50 offset:9472\n\t"
    "v_cvt_pk_bf16_f32 v17, v2, v3\n\t"
    "v_mfma_f32_16x16x32_bf16 v[12:15], a[112:115], a[16:19], v[28:31]\n\t"
    "v_exp_f32 v4, v4\n\t"
    "v_mfma_f32_16x16x32_bf16 v[12:15], a[116:119], a[20:23], v[12:15]\n\t"
    "v_exp_f32 v5, v5\n\t"
    "v_mfma_f32_16x16x32_bf16 v[12:15], a[120:123], a[24:27], v[12:15]\n\t"
    "v_exp_f32 v6, v6\n\t"
    "v_mfma_f32_16x16x32_bf16 v[12:15], a[124:127], a[28:31], v[12:15]\n\t"
    "v_exp_f32 v7, v7\n\t"
    "v_mfma_f32_16x16x32_bf16 v[0:3], a[96:99], a[32:35], v[32:35]\n\t"
    "v_cvt_pk_bf16_f32 v18, v4, v5\n\t"
    "v_cvt_pk_bf16_f32 v19, v6, v7\n\t"
    "v_mfma_f32_16x16x32_bf16 v[0:3], a[100:103], a[36:39], v[0:3]\n\t"
    "v_exp_f32 v8, v8\n\t"
    "v_mfma_f32_16x16x32_bf16 v[0:3], a[104:107], a[40:43], v[0:3]\n\t"
    "v_exp_f32 v9, v9\n\t"
    "v_mfma_f32_16x16x32_bf16 v[0:3], a[108:111], a[44:47], v[0:3]\n\t"
    "v_exp_f32 v10, v10\n\t"
    "v_mfma_f32_16x16x32_bf16 v[4:7], a[112:115], a[32:35], v[32:35]\n\t"
    "v_exp_f32 v11, v11\n\t"
    "v_mfma_f32_16x16x32_bf16 v[4:7], a[116:119], a[36:39], v[4:7]\n\t"
    "v_cvt_pk_bf16_f32 v20, v8, v9\n\t"
    "v_cvt_pk_bf16_f32 v21, v10, v11\n\t"
    "v_mfma_f32_16x16x32_bf16 v[4:7], a[120:123], a[40:43], v[4:7]\n\t"
    "v_exp_f32 v12, v12\n\t"
    "v_mfma_f32_16x16x32_bf16 v[4:7], a[124:127], a[44:47], v[4:7]\n\t"
    "ds_read_b64_tr_b16 a[150:151], v50 offset:13568\n\t"
    "v_exp_f32 v13, v13\n\t"
    "v_mfma_f32_16x16x32_bf16 v[8:11], a[96:99], a[48:51], v[36:39]\n\t"
    "ds_read_b64_tr_b16 a[152:153], v50 offset:9728\n\t"
    "v_exp_f32 v14, v14\n\t"
    "v_mfma_f32_16x16x32_bf16 v[8:11], a[100:103], a[52:55], v[8:11]\n\t"
    "ds_read_b64_tr_b16 a[154:155], v50 offset:13824\n\t"
    "v_exp_f32 v15, v15\n\t"
    "v_mfma_f32_16x16x32_bf16 v[8:11], a[104:107], a[56:59], v[8:11]\n\t"
    "ds_read_b64_tr_b16 a[156:157], v50 offset:9984\n\t"
    "v_cvt_pk_bf16_f32 v22, v12, v13\n\t"
    "v_cvt_pk_bf16_f32 v23, v14, v15\n\t"
    "v_mfma_f32_16x16x32_bf16 v[8:11], a[108:111], a[60:63], v[8:11]\n\t"
    "ds_read_b64_tr_b16 a[158:159], v50 offset:14080\n\t"
    "v_mfma_f32_16x16x32_bf16 v[12:15], a[112:115], a[48:51], v[36:39]\n\t"
    "v_exp_f32 v0, v0\n\t"
    "v_mfma_f32_16x16x32_bf16 v[12:15], a[116:119], a[52:55], v[12:15]\n\t"
    "v_exp_f32 v1, v1\n\t"
    "v_mfma_f32_16x16x32_bf16 v[12:15], a[120:123], a[56:59], v[12:15]\n\t"
    "v_exp_f32 v2, v2\n\t"
    "v_mfma_f32_16x16x32_bf16 v[12:15], a[124:127], a[60:63], v[12:15]\n\t"
    "v_exp_f32 v3, v3\n\t"
    "s_waitcnt lgkmcnt(3)\n\t"
    "v_mfma_f32_16x16x32_bf16 v[64:67], a[128:131], v[16:19], v[64:67]\n\t"
    "v_exp_f32 v4, v4\n\t"
    "v_mfma_f32_16x16x32_bf16 v[68:71], a[132:135], v[16:19], v[68:71]\n\t"
    "v_exp_f32 v5, v5\n\t"
    "v_mfma_f32_16x16x32_bf16 v[72:75], a[136:139], v[16:19], v[72:75]\n\t"
    "v_exp_f32 v6, v6\n\t"
    "v_mfma_f32_16x16x32_bf16 v[76:79], a[140:143], v[16:19], v[76:79]\n\t"
    "v_exp_f32 v7, v7\n\t"
    "v_mfma_f32_16x16x32_bf16 v[80:83], a[144:147], v[16:19], v[80:83]\n\t"
    "v_exp_f32 v8, v8\n\t"
    "v_mfma_f32_16x16x32_bf16 v[84:87], a[148:151], v[16:19], v[84:87]\n\t"
    "v_exp_f32 v9, v9\n\t"
    "s_waitcnt lgkmcnt(0)\n\t"
    "v_mfma_f32_16x16x32_bf16 v[88:91], a[152:155], v[16:19], v[88:91]\n\t"
    "v_exp_f32 v10, v10\n\t"
    "v_mfma_f32_16x16x32_bf16 v[92:95], a[156:159], v[16:19], v[92:95]\n\t"
    "v_exp_f32 v11, v11\n\t"
    "v_mfma_f32_16x16x32_bf16 a[160:163], a[184:187], v[16:19], a[160:163]\n\t"
    "v_cvt_pk_bf16_f32 v16, v0, v1\n\t"
    "v_cvt_pk_bf16_f32 v17, v2, v3\n\t"
    "v_mfma_f32_16x16x32_bf16 v[96:99], a[128:131], v[20:23], v[96:99]\n\t"
    "v_cvt_pk_bf16_f32 v18, v4, v5\n\t"
    "v_cvt_pk_bf16_f32 v19, v6, v7\n\t"
    "v_mfma_f32_16x16x32_bf16 v[100:103], a[132:135], v[20:23], v[100:103]\n\t"
    "v_exp_f32 v12, v12\n\t"
    "v_mfma_f32_16x16x32_bf16 v[104:107], a[136:139], v[20:23], v[104:107]\n\t"
    "v_exp_f32 v13, v13\n\t"
    "v_mfma_f32_16x16x32_bf16 v[108:111], a[140:143], v[20:23], v[108:111]\n\t"
    "v_exp_f32 v14, v14\n\t"
    "v_mfma_f32_16x16x32_bf16 v[112:115], a[144:147], v[20:23], v[112:115]\n\t"
    "v_exp_f32 v15, v15\n\t"
    "v_mfma_f32_16x16x32_bf16 v[116:119], a[148:151], v[20:23], v[116:119]\n\t"
    "v_mfma_f32_16x16x32_bf16 v[120:123], a[152:155], v[20:23], v[120:123]\n\t"
    "v_mfma_f32_16x16x32_bf16 v[124:127], a[156:159], v[20:23], v[124:127]\n\t"
    "v_mfma_f32_16x16x32_bf16 a[164:167], a[184:187], v[20:23], a[164:167]\n\t"
    "v_cvt_pk_bf16_f32 v20, v8, v9\n\t"
    "v_cvt_pk_bf16_f32 v21, v10, v11\n\t"
    "v_mfma_f32_16x16x32_bf16 v[0:3], a[96:99], a[64:67], v[40:43]\n\t"
    "v_cvt_pk_bf16_f32 v22, v12, v13\n\t"
    "v_cvt_pk_bf16_f32 v23, v14, v15\n\t"
    "v_mfma_f32_16x16x32_bf16 v[0:3], a[100:103], a[68:71], v[0:3]\n\t"
    "v_mfma_f32_16x16x32_bf16 v[0:3], a[104:107], a[72:75], v[0:3]\n\t"
    "v_mfma_f32_16x16x32_bf16 v[0:3], a[108:111], a[76:79], v[0:3]\n\t"
    "v_mfma_f32_16x16x32_bf16 v[4:7], a[112:115], a[64:67], v[40:43]\n\t"
    "v_mfma_f32_16x16x32_bf16 v[4:7], a[116:119], a[68:71], v[4:7]\n\t"
    "v_mfma_f32_16x16x32_bf16 v[4:7], a[120:123], a[72:75], v[4:7]\n\t"
    "v_mfma_f32_16x16x32_bf16 v[4:7], a[124:127], a[76:79], v[4:7]\n\t"
    "v_mfma_f32_16x16x32_bf16 v[8:11], a[96:99], a[80:83], v[44:47]\n\t"
    "v_mfma_f32_16x16x32_bf16 v[8:11], a[100:103], a[84:87], v[8:11]\n\t"
    "v_mfma_f32_16x16x32_bf16 v[8:11], a[104:107], a[88:91], v[8:11]\n\t"
    "v_mfma_f32_16x16x32_bf16 v[8:11], a[108:111], a[92:95], v[8:11]\n\t"
    "v_exp_f32 v0, v0\n\t"
    "v_mfma_f32_16x16x32_bf16 v[12:15], a[112:115], a[80:83], v[44:47]\n\t"
    "v_exp_f32 v1, v1\n\t"
    "v_mfma_f32_16x16x32_bf16 v[12:15], a[116:119], a[84:87], v[12:15]\n\t"
    "v_exp_f32 v2, v2\n\t"
    "v_mfma_f32_16x16x32_bf16 v[12:15], a[120:123], a[88:91], v[12:15]\n\t"
    "v_exp_f32 v3, v3\n\t"
    "v_mfma_f32_16x16x32_bf16 v[12:15], a[124:127], a[92:95], v[12:15]\n\t"
    "s_waitcnt vmcnt(6)\n\t"
    "s_barrier\n\t"
    "v_mfma_f32_16x16x32_bf16 v[128:131], a[128:131], v[16:19], v[128:131]\n\t"
    "ds_read_b128 a[96:99], v48 offset:16384\n\t"
    "ds_read_b128 a[100:103], v49 offset:16384\n\t"
    "v_mfma_f32_16x16x32_bf16 v[132:135], a[132:135], v[16:19], v[132:135]\n\t"
    "ds_read_b128 a[104:107], v48 offset:17408\n\t"
    "ds_read_b128 a[108:111], v49 offset:17408\n\t"
    "v_mfma_f32_16x16x32_bf16 v[136:139], a[136:139], v[16:19], v[136:139]\n\t"
    "ds_read_b128 a[112:115], v48 offset:20480\n\t"
    "ds_read_b128 a[116:119], v49 offset:20480\n\t"
    "v_mfma_f32_16x16x32_bf16 v[140:143], a[140:143], v[16:19], v[140:143]\n\t"
    "ds_read_b128 a[120:123], v48 offset:21504\n\t"
    "ds_read_b128 a[124:127], v49 offset:21504\n\t"
    "v_mfma_f32_16x16x32_bf16 v[144:147], a[144:147], v[16:19], v[144:147]\n\t"
    "v_exp_f32 v4, v4\n\t"
    "v_mfma_f32_16x16x32_bf16 v[148:151], a[148:151], v[16:19], v[148:151]\n\t"
    "v_exp_f32 v5, v5\n\t"
    "v_mfma_f32_16x16x32_bf16 v[152:155], a[152:155], v[16:19], v[152:155]\n\t"
    "v_exp_f32 v6, v6\n\t"
    "v_mfma_f32_16x16x32_bf16 v[156:159], a[156:159], v[16:19], v[156:159]\n\t"
    "v_exp_f32 v7, v7\n\t"
    "s_add_u32 m0, s54, 40960\n\t"
    "v_mfma_f32_16x16x32_bf16 a[168:171], a[184:187], v[16:19], a[168:171]\n\t"
    "v_cvt_pk_bf16_f32 v16, v0, v1\n\t"
    "v_cvt_pk_bf16_f32 v17, v2, v3\n\t"
    "buffer_load_dwordx4 v53, s[44:47], s58 offen nt lds\n\t"
    "v_mfma_f32_16x16x32_bf16 v[160:163], a[128:131], v[20:23], v[160:163]\n\t"
    "v_cvt_pk_bf16_f32 v18, v4, v5\n\t"
    "v_cvt_pk_bf16_f32 v19, v6, v7\n\t"
    "v_mfma_f32_16x16x32_bf16 v[164:167], a[132:135], v[20:23], v[164:167]\n\t"
    "v_exp_f32 v8, v8\n\t"
    "v_mfma_f32_16x16x32_bf16 v[168:171], a[136:139], v[20:23], v[168:171]\n\t"
    "v_exp_f32 v9, v9\n\t"
    "v_mfma_f32_16x16x32_bf16 v[172:175], a[140:143], v[20:23], v[172:175]\n\t"
    "v_exp_f32 v10, v10\n\t"
    "v_mfma_f32_16x16x32_bf16 v[176:179], a[144:147], v[20:23], v[176:179]\n\t"
    "v_exp_f32 v11, v11\n\t"
    "v_mfma_f32_16x16x32_bf16 v[180:183], a[148:151], v[20:23], v[180:183]\n\t"
    "v_exp_f32 v12, v12\n\t"
    "v_mfma_f32_16x16x32_bf16 v[184:187], a[152:155], v[20:23], v[184:187]\n\t"
    "v_exp_f32 v13, v13\n\t"
    "v_mfma_f32_16x16x32_bf16 v[188:191], a[156:159], v[20:23], v[188:191]\n\t"
    "v_exp_f32 v14, v14\n\t"
    "v_mfma_f32_16x16x32_bf16 a[172:175], a[184:187], v[20:23], a[172:175]\n\t"
    "v_cvt_pk_bf16_f32 v20, v8, v9\n\t"
    "v_cvt_pk_bf16_f32 v21, v10, v11\n\t"
    "buffer_load_dwordx4 v53, s[44:47], s58 offen offset:2048 nt lds\n\t"
    "s_waitcnt lgkmcnt(0)\n\t"
    "v_mfma_f32_16x16x32_bf16 v[0:3], a[96:99], a[0:3], v[24:27]\n\t"
    "v_exp_f32 v15, v15\n\t"
    "v_mfma_f32_16x16x32_bf16 v[0:3], a[100:103], a[4:7], v[0:3]\n\t"
    "v_cvt_pk_bf16_f32 v22, v12, v13\n\t"
    "v_cvt_pk_bf16_f32 v23, v14, v15\n\t"
    "v_mfma_f32_16x16x32_bf16 v[0:3], a[104:107], a[8:11], v[0:3]\n\t"
    "s_add_u32 m0, s53, 49152\n\t"
    "s_add_u32 s58, s52, 49152\n\t"
    "v_mfma_f32_16x16x32_bf16 v[0:3], a[108:111], a[12:15], v[0:3]\n\t"
    "buffer_load_dwordx4 v51, s[40:43], s58 offen nt lds\n\t"
    "v_mfma_f32_16x16x32_bf16 v[4:7], a[112:115], a[0:3], v[24:27]\n\t"
    "v_mfma_f32_16x16x32_bf16 v[4:7], a[116:119], a[4:7], v[4:7]\n\t"
    "v_mfma_f32_16x16x32_bf16 v[4:7], a[120:123], a[8:11], v[4:7]\n\t"
    "v_mfma_f32_16x16x32_bf16 v[4:7], a[124:127], a[12:15], v[4:7]\n\t"
    "v_mfma_f32_16x16x32_bf16 v[8:11], a[96:99], a[16:19], v[28:31]\n\t"
    "buffer_load_dwordx4 v52, s[40:43], s58 offen offset:2048 nt lds\n\t"
    "v_mfma_f32_16x16x32_bf16 v[8:11], a[100:103], a[20:23], v[8:11]\n\t"
    "v_exp_f32 v0, v0\n\t"
    "v_mfma_f32_16x16x32_bf16 v[8:11], a[104:107], a[24:27], v[8:11]\n\t"
    "v_exp_f32 v1, v1\n\t"
    "v_mfma_f32_16x16x32_bf16 v[8:11], a[108:111], a[28:31], v[8:11]\n\t"
    "v_exp_f32 v2, v2\n\t"
    "v_mfma_f32_16x16x32_bf16 v[12:15], a[112:115], a[16:19], v[28:31]\n\t"
    "v_exp_f32 v3, v3\n\t"
    "v_mfma_f32_16x16x32_bf16 v[12:15], a[116:119], a[20:23], v[12:15]\n\t"
    "v_exp_f32 v4, v4\n\t"
    "v_mfma_f32_16x16x32_bf16 v[12:15], a[120:123], a[24:27], v[12:15]\n\t"
    "v_exp_f32 v5, v5\n\t"
    "v_mfma_f32_16x16x32_bf16 v[12:15], a[124:127], a[28:31], v[12:15]\n\t"
    "v_exp_f32 v6, v6\n\t"
    "v_mfma_f32_16x16x32_bf16 v[192:195], a[128:131], v[16:19], v[192:195]\n\t"
    "v_exp_f32 v7, v7\n\t"
    "v_mfma_f32_16x16x32_bf16 v[196:199], a[132:135], v[16:19], v[196:199]\n\t"
    "v_exp_f32 v8, v8\n\t"
    "v_mfma_f32_16x16x32_bf16 v[200:203], a[136:139], v[16:19], v[200:203]\n\t"
    "v_exp_f32 v9, v9\n\t"
    "v_mfma_f32_16x16x32_bf16 v[204:207], a[140:143], v[16:19], v[204:207]\n\t"
    "v_exp_f32 v10, v10\n\t"
    "v_mfma_f32_16x16x32_bf16 v[208:211], a[144:147], v[16:19], v[208:211]\n\t"
    "v_exp_f32 v11, v11\n\t"
    "v_mfma_f32_16x16x32_bf16 v[212:215], a[148:151], v[16:19], v[212:215]\n\t"
    "v_exp_f32 v12, v12\n\t"
    "v_mfma_f32_16x16x32_bf16 v[216:219], a[152:155], v[16:19], v[216:219]\n\t"
    "v_exp_f32 v13, v13\n\t"
    "v_mfma_f32_16x16x32_bf16 v[220:223], a[156:159], v[16:19], v[220:223]\n\t"
    "v_exp_f32 v14, v14\n\t"
    "v_mfma_f32_16x16x32_bf16 a[176:179], a[184:187], v[16:19], a[176:179]\n\t"
    "v_cvt_pk_bf16_f32 v16, v0, v1\n\t"
    "v_cvt_pk_bf16_f32 v17, v2, v3\n\t"
    "v_mfma_f32_16x16x32_bf16 v[224:227], a[128:131], v[20:23], v[224:227]\n\t"
    "ds_read_b64_tr_b16 a[128:129], v50 offset:16384\n\t"
    "v_cvt_pk_bf16_f32 v18, v4, v5\n\t"
    "s_add_u32 m0, s54, 49152\n\t"
    "v_mfma_f32_16x16x32_bf16 v[228:231], a[132:135], v[20:23], v[228:231]\n\t"
    "ds_read_b64_tr_b16 a[130:131], v50 offset:20480\n\t"
    "v_cvt_pk_bf16_f32 v19, v6, v7\n\t"
    "buffer_load_dwordx4 v53, s[44:47], s58 offen nt lds\n\t"
    "v_mfma_f32_16x16x32_bf16 v[232:235], a[136:139], v[20:23], v[232:235]\n\t"
    "ds_read_b64_tr_b16 a[132:133], v50 offset:16640\n\t"
    "ds_read_b64_tr_b16 a[134:135], v50 offset:20736\n\t"
    "v_mfma_f32_16x16x32_bf16 v[236:239], a[140:143], v[20:23], v[236:239]\n\t"
    "ds_read_b64_tr_b16 a[136:137], v50 offset:16896\n\t"
    "ds_read_b64_tr_b16 a[138:139], v50 offset:20992\n\t"
    "v_mfma_f32_16x16x32_bf16 v[240:243], a[144:147], v[20:23], v[240:243]\n\t"
    "ds_read_b64_tr_b16 a[140:141], v50 offset:17152\n\t"
    "ds_read_b64_tr_b16 a[142:143], v50 offset:21248\n\t"
    "v_mfma_f32_16x16x32_bf16 v[244:247], a[148:151], v[20:23], v[244:247]\n\t"
    "v_exp_f32 v15, v15\n\t"
    "v_mfma_f32_16x16x32_bf16 v[248:251], a[152:155], v[20:23], v[248:251]\n\t"
    "ds_read_b64_tr_b16 a[144:145], v50 offset:17408\n\t"
    "ds_read_b64_tr_b16 a[146:147], v50 offset:21504\n\t"
    "v_mfma_f32_16x16x32_bf16 v[252:255], a[156:159], v[20:23], v[252:255]\n\t"
    "ds_read_b64_tr_b16 a[148:149], v50 offset:17664\n\t"
    "ds_read_b64_tr_b16 a[150:151], v50 offset:21760\n\t"
    "v_mfma_f32_16x16x32_bf16 a[180:183], a[184:187], v[20:23], a[180:183]\n\t"
    "ds_read_b64_tr_b16 a[152:153], v50 offset:17920\n\t"
    "v_cvt_pk_bf16_f32 v20, v8, v9\n\t"
    "v_mfma_f32_16x16x32_bf16 v[0:3], a[96:99], a[32:35], v[32:35]\n\t"
    "ds_read_b64_tr_b16 a[154:155], v50 offset:22016\n\t"
    "v_cvt_pk_bf16_f32 v21, v10, v11\n\t"
    "v_mfma_f32_16x16x32_bf16 v[0:3], a[100:103], a[36:39], v[0:3]\n\t"
    "ds_read_b64_tr_b16 a[156:157], v50 offset:18176\n\t"
    "v_cvt_pk_bf16_f32 v22, v12, v13\n\t"
    "buffer_load_dwordx4 v53, s[44:47], s58 offen offset:2048 nt lds\n\t"
    "v_mfma_f32_16x16x32_bf16 v[0:3], a[104:107], a[40:43], v[0:3]\n\t"
    "ds_read_b64_tr_b16 a[158:159], v50 offset:22272\n\t"
    "v_cvt_pk_bf16_f32 v23, v14, v15\n\t"
    "v_mfma_f32_16x16x32_bf16 v[0:3], a[108:111], a[44:47], v[0:3]\n\t"
    "s_add_u32 m0, s53, 57344\n\t"
    "s_add_u32 s58, s52, 57344\n\t"
    "v_mfma_f32_16x16x32_bf16 v[4:7], a[112:115], a[32:35], v[32:35]\n\t"
    "buffer_load_dwordx4 v51, s[40:43], s58 offen nt lds\n\t"
    "v_mfma_f32_16x16x32_bf16 v[4:7], a[116:119], a[36:39], v[4:7]\n\t"
    "v_mfma_f32_16x16x32_bf16 v[4:7], a[120:123], a[40:43], v[4:7]\n\t"
    "v_mfma_f32_16x16x32_bf16 v[4:7], a[124:127], a[44:47], v[4:7]\n\t"
    "v_mfma_f32_16x16x32_bf16 v[8:11], a[96:99], a[48:51], v[36:39]\n\t"
    "v_exp_f32 v0, v0\n\t"
    "v_mfma_f32_16x16x32_bf16 v[8:11], a[100:103], a[52:55], v[8:11]\n\t"
    "v_exp_f32 v1, v1\n\t"
    "v_mfma_f32_16x16x32_bf16 v[8:11], a[104:107], a[56:59], v[8:11]\n\t"
    "v_exp_f32 v2, v2\n\t"
    "v_mfma_f32_16x16x32_bf16 v[8:11], a[108:111], a[60:63], v[8:11]\n\t"
    "v_exp_f32 v3, v3\n\t"
    "v_mfma_f32_16x16x32_bf16 v[12:15], a[112:115], a[48:51], v[36:39]\n\t"
    "v_exp_f32 v4, v4\n\t"
    "v_mfma_f32_16x16x32_bf16 v[12:15], a[116:119], a[52:55], v[12:15]\n\t"
    "v_exp_f32 v5, v5\n\t"
    "v_mfma_f32_16x16x32_bf16 v[12:15], a[120:123], a[56:59], v[12:15]\n\t"
    "v_exp_f32 v6, v6\n\t"
    "v_mfma_f32_16x16x32_bf16 v[12:15], a[124:127], a[60:63], v[12:15]\n\t"
    "v_exp_f32 v7, v7\n\t"
    "s_waitcnt lgkmcnt(0)\n\t"
    "v_mfma_f32_16x16x32_bf16 v[64:67], a[128:131], v[16:19], v[64:67]\n\t"
    "v_exp_f32 v8, v8\n\t"
    "v_mfma_f32_16x16x32_bf16 v[68:71], a[132:135], v[16:19], v[68:71]\n\t"
    "v_exp_f32 v9, v9\n\t"
    "v_mfma_f32_16x16x32_bf16 v[72:75], a[136:139], v[16:19], v[72:75]\n\t"
    "v_exp_f32 v10, v10\n\t"
    "v_mfma_f32_16x16x32_bf16 v[76:79], a[140:143], v[16:19], v[76:79]\n\t"
    "v_exp_f32 v11, v11\n\t"
    "buffer_load_dwordx4 v52, s[40:43], s58 offen offset:2048 nt lds\n\t"
    "v_mfma_f32_16x16x32_bf16 v[80:83], a[144:147], v[16:19], v[80:83]\n\t"
    "v_exp_f32 v12, v12\n\t"
    "v_mfma_f32_16x16x32_bf16 v[84:87], a[148:151], v[16:19], v[84:87]\n\t"
    "v_exp_f32 v13, v13\n\t"
    "v_mfma_f32_16x16x32_bf16 v[88:91], a[152:155], v[16:19], v[88:91]\n\t"
    "v_exp_f32 v14, v14\n\t"
    "v_mfma_f32_16x16x32_bf16 v[92:95], a[156:159], v[16:19], v[92:95]\n\t"
    "v_exp_f32 v15, v15\n\t"
    "v_mfma_f32_16x16x32_bf16 a[160:163], a[184:187], v[16:19], a[160:163]\n\t"
    "v_cvt_pk_bf16_f32 v16, v0, v1\n\t"
    "v_cvt_pk_bf16_f32 v17, v2, v3\n\t"
    "v_mfma_f32_16x16x32_bf16 v[96:99], a[128:131], v[20:23], v[96:99]\n\t"
    "v_cvt_pk_bf16_f32 v18, v4, v5\n\t"
    "v_cvt_pk_bf16_f32 v19, v6, v7\n\t"
    "v_mfma_f32_16x16x32_bf16 v[100:103], a[132:135], v[20:23], v[100:103]\n\t"
    "s_add_u32 m0, s54, 57344\n\t"
    "v_mfma_f32_16x16x32_bf16 v[104:107], a[136:139], v[20:23], v[104:107]\n\t"
    "buffer_load_dwordx4 v53, s[44:47], s58 offen nt lds\n\t"
    "v_mfma_f32_16x16x32_bf16 v[108:111], a[140:143], v[20:23], v[108:111]\n\t"
    "buffer_load_dwordx4 v53, s[44:47], s58 offen offset:2048 nt lds\n\t"
    "v_mfma_f32_16x16x32_bf16 v[112:115], a[144:147], v[20:23], v[112:115]\n\t"
    "v_mfma_f32_16x16x32_bf16 v[116:119], a[148:151], v[20:23], v[116:119]\n\t"
    "v_mfma_f32_16x16x32_bf16 v[120:123], a[152:155], v[20:23], v[120:123]\n\t"
    "v_mfma_f32_16x16x32_bf16 v[124:127], a[156:159], v[20:23], v[124:127]\n\t"
    "v_mfma_f32_16x16x32_bf16 a[164:167], a[184:187], v[20:23], a[164:167]\n\t"
    "v_cvt_pk_bf16_f32 v20, v8, v9\n\t"
    "v_cvt_pk_bf16_f32 v21, v10, v11\n\t"
    "v_mfma_f32_16x16x32_bf16 v[0:3], a[96:99], a[64:67], v[40:43]\n\t"
    "v_cvt_pk_bf16_f32 v22, v12, v13\n\t"
    "v_cvt_pk_bf16_f32 v23, v14, v15\n\t"
    "v_mfma_f32_16x16x32_bf16 v[0:3], a[100:103], a[68:71], v[0:3]\n\t"
    "v_mfma_f32_16x16x32_bf16 v[0:3], a[104:107], a[72:75], v[0:3]\n\t"
    "v_mfma_f32_16x16x32_bf16 v[0:3], a[108:111], a[76:79], v[0:3]\n\t"
    "v_mfma_f32_16x16x32_bf16 v[4:7], a[112:115], a[64:67], v[40:43]\n\t"
    "v_mfma_f32_16x16x32_bf16 v[4:7], a[116:119], a[68:71], v[4:7]\n\t"
    "v_mfma_f32_16x16x32_bf16 v[4:7], a[120:123], a[72:75], v[4:7]\n\t"
    "v_mfma_f32_16x16x32_bf16 v[4:7], a[124:127], a[76:79], v[4:7]\n\t"
    "v_mfma_f32_16x16x32_bf16 v[8:11], a[96:99], a[80:83], v[44:47]\n\t"
    "ds_read_b128 a[96:99], v48 offset:24576\n\t"
    "v_mfma_f32_16x16x32_bf16 v[8:11], a[100:103], a[84:87], v[8:11]\n\t"
    "ds_read_b128 a[100:103], v49 offset:24576\n\t"
    "v_mfma_f32_16x16x32_bf16 v[8:11], a[104:107], a[88:91], v[8:11]\n\t"
    "ds_read_b128 a[104:107], v48 offset:25600\n\t"
    "v_mfma_f32_16x16x32_bf16 v[8:11], a[108:111], a[92:95], v[8:11]\n\t"
    "ds_read_b128 a[108:111], v49 offset:25600\n\t"
    "v_mfma_f32_16x16x32_bf16 v[12:15], a[112:115], a[80:83], v[44:47]\n\t"
    "ds_read_b128 a[112:115], v48 offset:28672\n\t"
    "v_mfma_f32_16x16x32_bf16 v[12:15], a[116:119], a[84:87], v[12:15]\n\t"
    "ds_read_b128 a[116:119], v49 offset:28672\n\t"
    "v_mfma_f32_16x16x32_bf16 v[12:15], a[120:123], a[88:91], v[12:15]\n\t"
    "ds_read_b128 a[120:123], v48 offset:29696\n\t"
    "v_mfma_f32_16x16x32_bf16 v[12:15], a[124:127], a[92:95], v[12:15]\n\t"
    "ds_read_b128 a[124:127], v49 offset:29696\n\t"
    "v_mfma_f32_16x16x32_bf16 v[128:131], a[128:131], v[16:19], v[128:131]\n\t"
    "v_exp_f32 v0, v0\n\t"
    "v_mfma_f32_16x16x32_bf16 v[132:135], a[132:135], v[16:19], v[132:135]\n\t"
    "v_exp_f32 v1, v1\n\t"
    "v_mfma_f32_16x16x32_bf16 v[136:139], a[136:139], v[16:19], v[136:139]\n\t"
    "v_exp_f32 v2, v2\n\t"
    "v_mfma_f32_16x16x32_bf16 v[140:143], a[140:143], v[16:19], v[140:143]\n\t"
    "v_exp_f32 v3, v3\n\t"
    "v_mfma_f32_16x16x32_bf16 v[144:147], a[144:147], v[16:19], v[144:147]\n\t"
    "v_exp_f32 v4, v4\n\t"
    "v_mfma_f32_16x16x32_bf16 v[148:151], a[148:151], v[16:19], v[148:151]\n\t"
    "v_exp_f32 v5, v5\n\t"
    "v_mfma_f32_16x16x32_bf16 v[152:155], a[152:155], v[16:19], v[152:155]\n\t"
    "v_exp_f32 v6, v6\n\t"
    "v_mfma_f32_16x16x32_bf16 v[156:159], a[156:159], v[16:19], v[156:159]\n\t"
    "v_exp_f32 v7, v7\n\t"
    "v_mfma_f32_16x16x32_bf16 a[168:171], a[184:187], v[16:19], a[168:171]\n\t"
    "v_cvt_pk_bf16_f32 v16, v0, v1\n\t"
    "v_cvt_pk_bf16_f32 v17, v2, v3\n\t"
    "v_mfma_f32_16x16x32_bf16 v[160:163], a[128:131], v[20:23], v[160:163]\n\t"
    "v_cvt_pk_bf16_f32 v18, v4, v5\n\t"
    "v_cvt_pk_bf16_f32 v19, v6, v7\n\t"
    "v_mfma_f32_16x16x32_bf16 v[164:167], a[132:135], v[20:23], v[164:167]\n\t"
    "v_exp_f32 v8, v8\n\t"
    "v_mfma_f32_16x16x32_bf16 v[168:171], a[136:139], v[20:23], v[168:171]\n\t"
    "v_exp_f32 v9, v9\n\t"
    "v_mfma_f32_16x16x32_bf16 v[172:175], a[140:143], v[20:23], v[172:175]\n\t"
    "v_exp_f32 v10, v10\n\t"
    "v_mfma_f32_16x16x32_bf16 v[176:179], a[144:147], v[20:23], v[176:179]\n\t"
    "v_exp_f32 v11, v11\n\t"
    "v_mfma_f32_16x16x32_bf16 v[180:183], a[148:151], v[20:23], v[180:183]\n\t"
    "v_exp_f32 v12, v12\n\t"
    "v_mfma_f32_16x16x32_bf16 v[184:187], a[152:155], v[20:23], v[184:187]\n\t"
    "v_exp_f32 v13, v13\n\t"
    "v_mfma_f32_16x16x32_bf16 v[188:191], a[156:159], v[20:23], v[188:191]\n\t"
    "v_exp_f32 v14, v14\n\t"
    "v_mfma_f32_16x16x32_bf16 a[172:175], a[184:187], v[20:23], a[172:175]\n\t"
    "v_cvt_pk_bf16_f32 v20, v8, v9\n\t"
    "v_cvt_pk_bf16_f32 v21, v10, v11\n\t"
    "s_waitcnt lgkmcnt(0)\n\t"
    "v_mfma_f32_16x16x32_bf16 v[0:3], a[96:99], a[0:3], v[24:27]\n\t"
    "v_exp_f32 v15, v15\n\t"
    "v_mfma_f32_16x16x32_bf16 v[0:3], a[100:103], a[4:7], v[0:3]\n\t"
    "v_cvt_pk_bf16_f32 v22, v12, v13\n\t"
    "v_cvt_pk_bf16_f32 v23, v14, v15\n\t"
    "v_mfma_f32_16x16x32_bf16 v[0:3], a[104:107], a[8:11], v[0:3]\n\t"
    "v_mfma_f32_16x16x32_bf16 v[0:3], a[108:111], a[12:15], v[0:3]\n\t"
    "v_mfma_f32_16x16x32_bf16 v[4:7], a[112:115], a[0:3], v[24:27]\n\t"
    "v_mfma_f32_16x16x32_bf16 v[4:7], a[116:119], a[4:7], v[4:7]\n\t"
    "v_mfma_f32_16x16x32_bf16 v[4:7], a[120:123], a[8:11], v[4:7]\n\t"
    "v_mfma_f32_16x16x32_bf16 v[4:7], a[124:127], a[12:15], v[4:7]\n\t"
    "v_mfma_f32_16x16x32_bf16 v[8:11], a[96:99], a[16:19], v[28:31]\n\t"
    "v_mfma_f32_16x16x32_bf16 v[8:11], a[100:103], a[20:23], v[8:11]\n\t"
    "v_mfma_f32_16x16x32_bf16 v[8:11], a[104:107], a[24:27], v[8:11]\n\t"
    "v_mfma_f32_16x16x32_bf16 v[8:11], a[108:111], a[28:31], v[8:11]\n\t"
    "v_exp_f32 v0, v0\n\t"
    "v_mfma_f32_16x16x32_bf16 v[12:15], a[112:115], a[16:19], v[28:31]\n\t"
    "v_exp_f32 v1, v1\n\t"
    "v_mfma_f32_16x16x32_bf16 v[12:15], a[116:119], a[20:23], v[12:15]\n\t"
    "v_exp_f32 v2, v2\n\t"
    "v_mfma_f32_16x16x32_bf16 v[12:15], a[120:123], a[24:27], v[12:15]\n\t"
    "v_exp_f32 v3, v3\n\t"
    "v_mfma_f32_16x16x32_bf16 v[12:15], a[124:127], a[28:31], v[12:15]\n\t"
    "v_exp_f32 v4, v4\n\t"
    "v_mfma_f32_16x16x32_bf16 v[192:195], a[128:131], v[16:19], v[192:195]\n\t"
    "v_exp_f32 v5, v5\n\t"
    "v_mfma_f32_16x16x32_bf16 v[196:199], a[132:135], v[16:19], v[196:199]\n\t"
    "v_exp_f32 v6, v6\n\t"
    "v_mfma_f32_16x16x32_bf16 v[200:203], a[136:139], v[16:19], v[200:203]\n\t"
    "v_exp_f32 v7, v7\n\t"
    "v_mfma_f32_16x16x32_bf16 v[204:207], a[140:143], v[16:19], v[204:207]\n\t"
    "v_exp_f32 v8, v8\n\t"
    "v_mfma_f32_16x16x32_bf16 v[208:211], a[144:147], v[16:19], v[208:211]\n\t"
    "v_exp_f32 v9, v9\n\t"
    "v_mfma_f32_16x16x32_bf16 v[212:215], a[148:151], v[16:19], v[212:215]\n\t"
    "v_exp_f32 v10, v10\n\t"
    "v_mfma_f32_16x16x32_bf16 v[216:219], a[152:155], v[16:19], v[216:219]\n\t"
    "v_exp_f32 v11, v11\n\t"
    "v_mfma_f32_16x16x32_bf16 v[220:223], a[156:159], v[16:19], v[220:223]\n\t"
    "v_exp_f32 v12, v12\n\t"
    "v_mfma_f32_16x16x32_bf16 a[176:179], a[184:187], v[16:19], a[176:179]\n\t"
    "v_cvt_pk_bf16_f32 v16, v0, v1\n\t"
    "v_cvt_pk_bf16_f32 v17, v2, v3\n\t"
    "v_mfma_f32_16x16x32_bf16 v[224:227], a[128:131], v[20:23], v[224:227]\n\t"
    "ds_read_b64_tr_b16 a[128:129], v50 offset:24576\n\t"
    "v_cvt_pk_bf16_f32 v18, v4, v5\n\t"
    "v_mfma_f32_16x16x32_bf16 v[228:231], a[132:135], v[20:23], v[228:231]\n\t"
    "ds_read_b64_tr_b16 a[130:131], v50 offset:28672\n\t"
    "v_cvt_pk_bf16_f32 v19, v6, v7\n\t"
    "v_mfma_f32_16x16x32_bf16 v[232:235], a[136:139], v[20:23], v[232:235]\n\t"
    "ds_read_b64_tr_b16 a[132:133], v50 offset:24832\n\t"
    "ds_read_b64_tr_b16 a[134:135], v50 offset:28928\n\t"
    "v_mfma_f32_16x16x32_bf16 v[236:239], a[140:143], v[20:23], v[236:239]\n\t"
    "ds_read_b64_tr_b16 a[136:137], v50 offset:25088\n\t"
    "ds_read_b64_tr_b16 a[138:139], v50 offset:29184\n\t"
    "v_mfma_f32_16x16x32_bf16 v[240:243], a[144:147], v[20:23], v[240:243]\n\t"
    "ds_read_b64_tr_b16 a[140:141], v50 offset:25344\n\t"
    "ds_read_b64_tr_b16 a[142:143], v50 offset:29440\n\t"
    "v_mfma_f32_16x16x32_bf16 v[244:247], a[148:151], v[20:23], v[244:247]\n\t"
    "v_exp_f32 v13, v13\n\t"
    "v_mfma_f32_16x16x32_bf16 v[248:251], a[152:155], v[20:23], v[248:251]\n\t"
    "v_exp_f32 v14, v14\n\t"
    "v_mfma_f32_16x16x32_bf16 v[252:255], a[156:159], v[20:23], v[252:255]\n\t"
    "v_exp_f32 v15, v15\n\t"
    "v_mfma_f32_16x16x32_bf16 a[180:183], a[184:187], v[20:23], a[180:183]\n\t"
    "ds_read_b64_tr_b16 a[144:145], v50 offset:25600\n\t"
    "v_cvt_pk_bf16_f32 v20, v8, v9\n\t"
    "v_mfma_f32_16x16x32_bf16 v[0:3], a[96:99], a[32:35], v[32:35]\n\t"
    "ds_read_b64_tr_b16 a[146:147], v50 offset:29696\n\t"
    "v_cvt_pk_bf16_f32 v21, v10, v11\n\t"
    "v_mfma_f32_16x16x32_bf16 v[0:3], a[100:103], a[36:39], v[0:3]\n\t"
    "ds_read_b64_tr_b16 a[148:149], v50 offset:25856\n\t"
    "v_cvt_pk_bf16_f32 v22, v12, v13\n\t"
    "v_mfma_f32_16x16x32_bf16 v[0:3], a[104:107], a[40:43], v[0:3]\n\t"
    "ds_read_b64_tr_b16 a[150:151], v50 offset:29952\n\t"
    "v_cvt_pk_bf16_f32 v23, v14, v15\n\t"
    "v_mfma_f32_16x16x32_bf16 v[0:3], a[108:111], a[44:47], v[0:3]\n\t"
    "ds_read_b64_tr_b16 a[152:153], v50 offset:26112\n\t"
    "ds_read_b64_tr_b16 a[154:155], v50 offset:30208\n\t"
    "v_mfma_f32_16x16x32_bf16 v[4:7], a[112:115], a[32:35], v[32:35]\n\t"
    "ds_read_b64_tr_b16 a[156:157], v50 offset:26368\n\t"
    "ds_read_b64_tr_b16 a[158:159], v50 offset:30464\n\t"
    "v_mfma_f32_16x16x32_bf16 v[4:7], a[116:119], a[36:39], v[4:7]\n\t"
    "v_mfma_f32_16x16x32_bf16 v[4:7], a[120:123], a[40:43], v[4:7]\n\t"
    "v_mfma_f32_16x16x32_bf16 v[4:7], a[124:127], a[44:47], v[4:7]\n\t"
    "v_exp_f32 v0, v0\n\t"
    "v_mfma_f32_16x16x32_bf16 v[8:11], a[96:99], a[48:51], v[36:39]\n\t"
    "v_exp_f32 v1, v1\n\t"
    "v_mfma_f32_16x16x32_bf16 v[8:11], a[100:103], a[52:55], v[8:11]\n\t"
    "v_exp_f32 v2, v2\n\t"
    "v_mfma_f32_16x16x32_bf16 v[8:11], a[104:107], a[56:59], v[8:11]\n\t"
    "v_exp_f32 v3, v3\n\t"
    "v_mfma_f32_16x16x32_bf16 v[8:11], a[108:111], a[60:63], v[8:11]\n\t"
    "v_exp_f32 v4, v4\n\t"
    "v_mfma_f32_16x16x32_bf16 v[12:15], a[112:115], a[48:51], v[36:39]\n\t"
    "v_exp_f32 v5, v5\n\t"
    "v_mfma_f32_16x16x32_bf16 v[12:15], a[116:119], a[52:55], v[12:15]\n\t"
    "v_exp_f32 v6, v6\n\t"
    "v_mfma_f32_16x16x32_bf16 v[12:15], a[120:123], a[56:59], v[12:15]\n\t"
    "v_exp_f32 v7, v7\n\t"
    "v_mfma_f32_16x16x32_bf16 v[12:15], a[124:127], a[60:63], v[12:15]\n\t"
    "v_exp_f32 v8, v8\n\t"
    "s_waitcnt lgkmcnt(0)\n\t"
    "v_mfma_f32_16x16x32_bf16 v[64:67], a[128:131], v[16:19], v[64:67]\n\t"
    "v_exp_f32 v9, v9\n\t"
    "v_mfma_f32_16x16x32_bf16 v[68:71], a[132:135], v[16:19], v[68:71]\n\t"
    "v_exp_f32 v10, v10\n\t"
    "v_mfma_f32_16x16x32_bf16 v[72:75], a[136:139], v[16:19], v[72:75]\n\t"
    "v_exp_f32 v11, v11\n\t"
    "v_mfma_f32_16x16x32_bf16 v[76:79], a[140:143], v[16:19], v[76:79]\n\t"
    "v_exp_f32 v12, v12\n\t"
    "v_mfma_f32_16x16x32_bf16 v[80:83], a[144:147], v[16:19], v[80:83]\n\t"
    "v_exp_f32 v13, v13\n\t"
    "v_mfma_f32_16x16x32_bf16 v[84:87], a[148:151], v[16:19], v[84:87]\n\t"
    "v_exp_f32 v14, v14\n\t"
    "v_mfma_f32_16x16x32_bf16 v[88:91], a[152:155], v[16:19], v[88:91]\n\t"
    "v_exp_f32 v15, v15\n\t"
    "v_mfma_f32_16x16x32_bf16 v[92:95], a[156:159], v[16:19], v[92:95]\n\t"
    "v_mfma_f32_16x16x32_bf16 a[160:163], a[184:187], v[16:19], a[160:163]\n\t"
    "v_cvt_pk_bf16_f32 v16, v0, v1\n\t"
    "v_cvt_pk_bf16_f32 v17, v2, v3\n\t"
    "v_mfma_f32_16x16x32_bf16 v[96:99], a[128:131], v[20:23], v[96:99]\n\t"
    "v_cvt_pk_bf16_f32 v18, v4, v5\n\t"
    "v_cvt_pk_bf16_f32 v19, v6, v7\n\t"
    "v_mfma_f32_16x16x32_bf16 v[100:103], a[132:135], v[20:23], v[100:103]\n\t"
    "v_mfma_f32_16x16x32_bf16 v[104:107], a[136:139], v[20:23], v[104:107]\n\t"
    "v_mfma_f32_16x16x32_bf16 v[108:111], a[140:143], v[20:23], v[108:111]\n\t"
    "v_mfma_f32_16x16x32_bf16 v[112:115], a[144:147], v[20:23], v[112:115]\n\t"
    "v_mfma_f32_16x16x32_bf16 v[116:119], a[148:151], v[20:23], v[116:119]\n\t"
    "v_mfma_f32_16x16x32_bf16 v[120:123], a[152:155], v[20:23], v[120:123]\n\t"
    "v_mfma_f32_16x16x32_bf16 v[124:127], a[156:159], v[20:23], v[124:127]\n\t"
    "v_mfma_f32_16x16x32_bf16 a[164:167], a[184:187], v[20:23], a[164:167]\n\t"
    "v_cvt_pk_bf16_f32 v20, v8, v9\n\t"
    "v_cvt_pk_bf16_f32 v21, v10, v11\n\t"
    "v_mfma_f32_16x16x32_bf16 v[0:3], a[96:99], a[64:67], v[40:43]\n\t"
    "v_cvt_pk_bf16_f32 v22, v12, v13\n\t"
    "v_cvt_pk_bf16_f32 v23, v14, v15\n\t"
    "v_mfma_f32_16x16x32_bf16 v[0:3], a[100:103], a[68:71], v[0:3]\n\t"
    "v_mfma_f32_16x16x32_bf16 v[0:3], a[104:107], a[72:75], v[0:3]\n\t"
    "v_mfma_f32_16x16x32_bf16 v[0:3], a[108:111], a[76:79], v[0:3]\n\t"
    "v_mfma_f32_16x16x32_bf16 v[4:7], a[112:115], a[64:67], v[40:43]\n\t"
    "v_mfma_f32_16x16x32_bf16 v[4:7], a[116:119], a[68:71], v[4:7]\n\t"
    "v_mfma_f32_16x16x32_bf16 v[4:7], a[120:123], a[72:75], v[4:7]\n\t"
    "v_mfma_f32_16x16x32_bf16 v[4:7], a[124:127], a[76:79], v[4:7]\n\t"
    "v_mfma_f32_16x16x32_bf16 v[8:11], a[96:99], a[80:83], v[44:47]\n\t"
    "v_mfma_f32_16x16x32_bf16 v[8:11], a[100:103], a[84:87], v[8:11]\n\t"
    "v_mfma_f32_16x16x32_bf16 v[8:11], a[104:107], a[88:91], v[8:11]\n\t"
    "v_mfma_f32_16x16x32_bf16 v[8:11], a[108:111], a[92:95], v[8:11]\n\t"
    "v_exp_f32 v0, v0\n\t"
    "v_mfma_f32_16x16x32_bf16 v[12:15], a[112:115], a[80:83], v[44:47]\n\t"
    "v_exp_f32 v1, v1\n\t"
    "v_mfma_f32_16x16x32_bf16 v[12:15], a[116:119], a[84:87], v[12:15]\n\t"
    "v_exp_f32 v2, v2\n\t"
    "v_mfma_f32_16x16x32_bf16 v[12:15], a[120:123], a[88:91], v[12:15]\n\t"
    "v_exp_f32 v3, v3\n\t"
    "v_mfma_f32_16x16x32_bf16 v[12:15], a[124:127], a[92:95], v[12:15]\n\t"
    "s_waitcnt vmcnt(8)\n\t"
    "s_barrier\n\t"
    "v_mfma_f32_16x16x32_bf16 v[128:131], a[128:131], v[16:19], v[128:131]\n\t"
    "ds_read_b128 a[96:99], v48 offset:32768\n\t"
    "ds_read_b128 a[100:103], v49 offset:32768\n\t"
    "v_mfma_f32_16x16x32_bf16 v[132:135], a[132:135], v[16:19], v[132:135]\n\t"
    "ds_read_b128 a[104:107], v48 offset:33792\n\t"
    "ds_read_b128 a[108:111], v49 offset:33792\n\t"
    "v_mfma_f32_16x16x32_bf16 v[136:139], a[136:139], v[16:19], v[136:139]\n\t"
    "ds_read_b128 a[112:115], v48 offset:36864\n\t"
    "ds_read_b128 a[116:119], v49 offset:36864\n\t"
    "v_mfma_f32_16x16x32_bf16 v[140:143], a[140:143], v[16:19], v[140:143]\n\t"
    "ds_read_b128 a[120:123], v48 offset:37888\n\t"
    "ds_read_b128 a[124:127], v49 offset:37888\n\t"
    "v_mfma_f32_16x16x32_bf16 v[144:147], a[144:147], v[16:19], v[144:147]\n\t"
    "v_exp_f32 v4, v4\n\t"
    "v_mfma_f32_16x16x32_bf16 v[148:151], a[148:151], v[16:19], v[148:151]\n\t"
    "v_exp_f32 v5, v5\n\t"
    "v_mfma_f32_16x16x32_bf16 v[152:155], a[152:155], v[16:19], v[152:155]\n\t"
    "v_exp_f32 v6, v6\n\t"
    "v_mfma_f32_16x16x32_bf16 v[156:159], a[156:159], v[16:19], v[156:159]\n\t"
    "v_exp_f32 v7, v7\n\t"
    "s_add_u32 m0, s53, 0\n\t"
    "s_add_u32 s58, s52, 65536\n\t"
    "v_mfma_f32_16x16x32_bf16 a[168:171], a[184:187], v[16:19], a[168:171]\n\t"
    "v_cvt_pk_bf16_f32 v16, v0, v1\n\t"
    "v_cvt_pk_bf16_f32 v17, v2, v3\n\t"
    "buffer_load_dwordx4 v51, s[40:43], s58 offen nt lds\n\t"
    "v_mfma_f32_16x16x32_bf16 v[160:163], a[128:131], v[20:23], v[160:163]\n\t"
    "v_cvt_pk_bf16_f32 v18, v4, v5\n\t"
    "v_cvt_pk_bf16_f32 v19, v6, v7\n\t"
    "v_mfma_f32_16x16x32_bf16 v[164:167], a[132:135], v[20:23], v[164:167]\n\t"
    "v_exp_f32 v8, v8\n\t"
    "v_mfma_f32_16x16x32_bf16 v[168:171], a[136:139], v[20:23], v[168:171]\n\t"
    "v_exp_f32 v9, v9\n\t"
    "v_mfma_f32_16x16x32_bf16 v[172:175], a[140:143], v[20:23], v[172:175]\n\t"
    "v_exp_f32 v10, v10\n\t"
    "v_mfma_f32_16x16x32_bf16 v[176:179], a[144:147], v[20:23], v[176:179]\n\t"
    "v_exp_f32 v11, v11\n\t"
    "v_mfma_f32_16x16x32_bf16 v[180:183], a[148:151], v[20:23], v[180:183]\n\t"
    "v_exp_f32 v12, v12\n\t"
    "v_mfma_f32_16x16x32_bf16 v[184:187], a[152:155], v[20:23], v[184:187]\n\t"
    "v_exp_f32 v13, v13\n\t"
    "v_mfma_f32_16x16x32_bf16 v[188:191], a[156:159], v[20:23], v[188:191]\n\t"
    "v_exp_f32 v14, v14\n\t"
    "v_mfma_f32_16x16x32_bf16 a[172:175], a[184:187], v[20:23], a[172:175]\n\t"
    "v_cvt_pk_bf16_f32 v20, v8, v9\n\t"
    "v_cvt_pk_bf16_f32 v21, v10, v11\n\t"
    "buffer_load_dwordx4 v52, s[40:43], s58 offen offset:2048 nt lds\n\t"
    "s_waitcnt lgkmcnt(0)\n\t"
    "v_mfma_f32_16x16x32_bf16 v[0:3], a[96:99], a[0:3], v[24:27]\n\t"
    "v_exp_f32 v15, v15\n\t"
    "v_mfma_f32_16x16x32_bf16 v[0:3], a[100:103], a[4:7], v[0:3]\n\t"
    "v_cvt_pk_bf16_f32 v22, v12, v13\n\t"
    "v_cvt_pk_bf16_f32 v23, v14, v15\n\t"
    "v_mfma_f32_16x16x32_bf16 v[0:3], a[104:107], a[8:11], v[0:3]\n\t"
    "s_add_u32 m0, s54, 0\n\t"
    "v_mfma_f32_16x16x32_bf16 v[0:3], a[108:111], a[12:15], v[0:3]\n\t"
    "buffer_load_dwordx4 v53, s[44:47], s58 offen nt lds\n\t"
    "v_mfma_f32_16x16x32_bf16 v[4:7], a[112:115], a[0:3], v[24:27]\n\t"
    "v_mfma_f32_16x16x32_bf16 v[4:7], a[116:119], a[4:7], v[4:7]\n\t"
    "v_mfma_f32_16x16x32_bf16 v[4:7], a[120:123], a[8:11], v[4:7]\n\t"
    "v_mfma_f32_16x16x32_bf16 v[4:7], a[124:127], a[12:15], v[4:7]\n\t"
    "v_mfma_f32_16x16x32_bf16 v[8:11], a[96:99], a[16:19], v[28:31]\n\t"
    "buffer_load_dwordx4 v53, s[44:47], s58 offen offset:2048 nt lds\n\t"
    "v_mfma_f32_16x16x32_bf16 v[8:11], a[100:103], a[20:23], v[8:11]\n\t"
    "v_exp_f32 v0, v0\n\t"
    "v_mfma_f32_16x16x32_bf16 v[8:11], a[104:107], a[24:27], v[8:11]\n\t"
    "v_exp_f32 v1, v1\n\t"
    "v_mfma_f32_16x16x32_bf16 v[8:11], a[108:111], a[28:31], v[8:11]\n\t"
    "v_exp_f32 v2, v2\n\t"
    "v_mfma_f32_16x16x32_bf16 v[12:15], a[112:115], a[16:19], v[28:31]\n\t"
    "v_exp_f32 v3, v3\n\t"
    "v_mfma_f32_16x16x32_bf16 v[12:15], a[116:119], a[20:23], v[12:15]\n\t"
    "v_exp_f32 v4, v4\n\t"
    "v_mfma_f32_16x16x32_bf16 v[12:15], a[120:123], a[24:27], v[12:15]\n\t"
    "v_exp_f32 v5, v5\n\t"
    "v_mfma_f32_16x16x32_bf16 v[12:15], a[124:127], a[28:31], v[12:15]\n\t"
    "v_exp_f32 v6, v6\n\t"
    "v_mfma_f32_16x16x32_bf16 v[192:195], a[128:131], v[16:19], v[192:195]\n\t"
    "v_exp_f32 v7, v7\n\t"
    "v_mfma_f32_16x16x32_bf16 v[196:199], a[132:135], v[16:19], v[196:199]\n\t"
    "v_exp_f32 v8, v8\n\t"
    "v_mfma_f32_16x16x32_bf16 v[200:203], a[136:139], v[16:19], v[200:203]\n\t"
    "v_exp_f32 v9, v9\n\t"
    "v_mfma_f32_16x16x32_bf16 v[204:207], a[140:143], v[16:19], v[204:207]\n\t"
    "v_exp_f32 v10, v10\n\t"
    "v_mfma_f32_16x16x32_bf16 v[208:211], a[144:147], v[16:19], v[208:211]\n\t"
    "v_exp_f32 v11, v11\n\t"
    "v_mfma_f32_16x16x32_bf16 v[212:215], a[148:151], v[16:19], v[212:215]\n\t"
    "v_exp_f32 v12, v12\n\t"
    "v_mfma_f32_16x16x32_bf16 v[216:219], a[152:155], v[16:19], v[216:219]\n\t"
    "v_exp_f32 v13, v13\n\t"
    "v_mfma_f32_16x16x32_bf16 v[220:223], a[156:159], v[16:19], v[220:223]\n\t"
    "v_exp_f32 v14, v14\n\t"
    "v_mfma_f32_16x16x32_bf16 a[176:179], a[184:187], v[16:19], a[176:179]\n\t"
    "v_cvt_pk_bf16_f32 v16, v0, v1\n\t"
    "v_cvt_pk_bf16_f32 v17, v2, v3\n\t"
    "v_mfma_f32_16x16x32_bf16 v[224:227], a[128:131], v[20:23], v[224:227]\n\t"
    "ds_read_b64_tr_b16 a[128:129], v50 offset:32768\n\t"
    "v_cvt_pk_bf16_f32 v18, v4, v5\n\t"
    "s_add_u32 m0, s53, 8192\n\t"
    "s_add_u32 s58, s52, 73728\n\t"
    "v_mfma_f32_16x16x32_bf16 v[228:231], a[132:135], v[20:23], v[228:231]\n\t"
    "ds_read_b64_tr_b16 a[130:131], v50 offset:36864\n\t"
    "v_cvt_pk_bf16_f32 v19, v6, v7\n\t"
    "buffer_load_dwordx4 v51, s[40:43], s58 offen nt lds\n\t"
    "v_mfma_f32_16x16x32_bf16 v[232:235], a[136:139], v[20:23], v[232:235]\n\t"
    "ds_read_b64_tr_b16 a[132:133], v50 offset:33024\n\t"
    "ds_read_b64_tr_b16 a[134:135], v50 offset:37120\n\t"
    "v_mfma_f32_16x16x32_bf16 v[236:239], a[140:143], v[20:23], v[236:239]\n\t"
    "ds_read_b64_tr_b16 a[136:137], v50 offset:33280\n\t"
    "ds_read_b64_tr_b16 a[138:139], v50 offset:37376\n\t"
    "v_mfma_f32_16x16x32_bf16 v[240:243], a[144:147], v[20:23], v[240:243]\n\t"
    "ds_read_b64_tr_b16 a[140:141], v50 offset:33536\n\t"
    "ds_read_b64_tr_b16 a[142:143], v50 offset:37632\n\t"
    "v_mfma_f32_16x16x32_bf16 v[244:247], a[148:151], v[20:23], v[244:247]\n\t"
    "v_exp_f32 v15, v15\n\t"
    "v_mfma_f32_16x16x32_bf16 v[248:251], a[152:155], v[20:23], v[248:251]\n\t"
    "ds_read_b64_tr_b16 a[144:145], v50 offset:33792\n\t"
    "ds_read_b64_tr_b16 a[146:147], v50 offset:37888\n\t"
    "v_mfma_f32_16x16x32_bf16 v[252:255], a[156:159], v[20:23], v[252:255]\n\t"
    "ds_read_b64_tr_b16 a[148:149], v50 offset:34048\n\t"
    "ds_read_b64_tr_b16 a[150:151], v50 offset:38144\n\t"
    "v_mfma_f32_16x16x32_bf16 a[180:183], a[184:187], v[20:23], a[180:183]\n\t"
    "ds_read_b64_tr_b16 a[152:153], v50 offset:34304\n\t"
    "v_cvt_pk_bf16_f32 v20, v8, v9\n\t"
    "v_mfma_f32_16x16x32_bf16 v[0:3], a[96:99], a[32:35], v[32:35]\n\t"
    "ds_read_b64_tr_b16 a[154:155], v50 offset:38400\n\t"
    "v_cvt_pk_bf16_f32 v21, v10, v11\n\t"
    "v_mfma_f32_16x16x32_bf16 v[0:3], a[100:103], a[36:39], v[0:3]\n\t"
    "ds_read_b64_tr_b16 a[156:157], v50 offset:34560\n\t"
    "v_cvt_pk_bf16_f32 v22, v12, v13\n\t"
    "buffer_load_dwordx4 v52, s[40:43], s58 offen offset:2048 nt lds\n\t"
    "v_mfma_f32_16x16x32_bf16 v[0:3], a[104:107], a[40:43], v[0:3]\n\t"
    "ds_read_b64_tr_b16 a[158:159], v50 offset:38656\n\t"
    "v_cvt_pk_bf16_f32 v23, v14, v15\n\t"
    "v_mfma_f32_16x16x32_bf16 v[0:3], a[108:111], a[44:47], v[0:3]\n\t"
    "s_add_u32 m0, s54, 8192\n\t"
    "v_mfma_f32_16x16x32_bf16 v[4:7], a[112:115], a[32:35], v[32:35]\n\t"
    "buffer_load_dwordx4 v53, s[44:47], s58 offen nt lds\n\t"
    "v_mfma_f32_16x16x32_bf16 v[4:7], a[116:119], a[36:39], v[4:7]\n\t"
    "v_mfma_f32_16x16x32_bf16 v[4:7], a[120:123], a[40:43], v[4:7]\n\t"
    "v_mfma_f32_16x16x32_bf16 v[4:7], a[124:127], a[44:47], v[4:7]\n\t"
    "v_mfma_f32_16x16x32_bf16 v[8:11], a[96:99], a[48:51], v[36:39]\n\t"
    "v_mfma_f32_16x16x32_bf16 v[8:11], a[100:103], a[52:55], v[8:11]\n\t"
    "v_exp_f32 v0, v0\n\t"
    "v_mfma_f32_16x16x32_bf16 v[8:11], a[104:107], a[56:59], v[8:11]\n\t"
    "v_exp_f32 v1, v1\n\t"
    "v_mfma_f32_16x16x32_bf16 v[8:11], a[108:111], a[60:63], v[8:11]\n\t"
    "v_exp_f32 v2, v2\n\t"
    "v_mfma_f32_16x16x32_bf16 v[12:15], a[112:115], a[48:51], v[36:39]\n\t"
    "v_exp_f32 v3, v3\n\t"
    "v_mfma_f32_16x16x32_bf16 v[12:15], a[116:119], a[52:55], v[12:15]\n\t"
    "v_exp_f32 v4, v4\n\t"
    "v_mfma_f32_16x16x32_bf16 v[12:15], a[120:123], a[56:59], v[12:15]\n\t"
    "v_exp_f32 v5, v5\n\t"
    "v_mfma_f32_16x16x32_bf16 v[12:15], a[124:127], a[60:63], v[12:15]\n\t"
    "v_exp_f32 v6, v6\n\t"
    "s_waitcnt lgkmcnt(0)\n\t"
    "v_mfma_f32_16x16x32_bf16 v[64:67], a[128:131], v[16:19], v[64:67]\n\t"
    "v_exp_f32 v7, v7\n\t"
    "v_mfma_f32_16x16x32_bf16 v[68:71], a[132:135], v[16:19], v[68:71]\n\t"
    "v_exp_f32 v8, v8\n\t"
    "v_mfma_f32_16x16x32_bf16 v[72:75], a[136:139], v[16:19], v[72:75]\n\t"
    "v_exp_f32 v9, v9\n\t"
    "v_mfma_f32_16x16x32_bf16 v[76:79], a[140:143], v[16:19], v[76:79]\n\t"
    "v_exp_f32 v10, v10\n\t"
    "buffer_load_dwordx4 v53, s[44:47], s58 offen offset:2048 nt lds\n\t"
    "v_mfma_f32_16x16x32_bf16 v[80:83], a[144:147], v[16:19], v[80:83]\n\t"
    "v_exp_f32 v11, v11\n\t"
    "v_mfma_f32_16x16x32_bf16 v[84:87], a[148:151], v[16:19], v[84:87]\n\t"
    "v_exp_f32 v12, v12\n\t"
    "v_mfma_f32_16x16x32_bf16 v[88:91], a[152:155], v[16:19], v[88:91]\n\t"
    "v_exp_f32 v13, v13\n\t"
    "v_mfma_f32_16x16x32_bf16 v[92:95], a[156:159], v[16:19], v[92:95]\n\t"
    "v_exp_f32 v14, v14\n\t"
    "v_mfma_f32_16x16x32_bf16 a[160:163], a[184:187], v[16:19], a[160:163]\n\t"
    "v_cvt_pk_bf16_f32 v16, v0, v1\n\t"
    "v_cvt_pk_bf16_f32 v17, v2, v3\n\t"
    "v_mfma_f32_16x16x32_bf16 v[96:99], a[128:131], v[20:23], v[96:99]\n\t"
    "v_cvt_pk_bf16_f32 v18, v4, v5\n\t"
    "v_cvt_pk_bf16_f32 v19, v6, v7\n\t"
    "v_mfma_f32_16x16x32_bf16 v[100:103], a[132:135], v[20:23], v[100:103]\n\t"
    "v_exp_f32 v15, v15\n\t"
    "v_mfma_f32_16x16x32_bf16 v[104:107], a[136:139], v[20:23], v[104:107]\n\t"
    "v_mfma_f32_16x16x32_bf16 v[108:111], a[140:143], v[20:23], v[108:111]\n\t"
    "v_mfma_f32_16x16x32_bf16 v[112:115], a[144:147], v[20:23], v[112:115]\n\t"
    "v_mfma_f32_16x16x32_bf16 v[116:119], a[148:151], v[20:23], v[116:119]\n\t"
    "v_mfma_f32_16x16x32_bf16 v[120:123], a[152:155], v[20:23], v[120:123]\n\t"
    "v_mfma_f32_16x16x32_bf16 v[124:127], a[156:159], v[20:23], v[124:127]\n\t"
    "v_mfma_f32_16x16x32_bf16 a[164:167], a[184:187], v[20:23], a[164:167]\n\t"
    "v_cvt_pk_bf16_f32 v20, v8, v9\n\t"
    "v_cvt_pk_bf16_f32 v21, v10, v11\n\t"
    "v_mfma_f32_16x16x32_bf16 v[0:3], a[96:99], a[64:67], v[40:43]\n\t"
    "v_cvt_pk_bf16_f32 v22, v12, v13\n\t"
    "v_cvt_pk_bf16_f32 v23, v14, v15\n\t"
    "v_mfma_f32_16x16x32_bf16 v[0:3], a[100:103], a[68:71], v[0:3]\n\t"
    "v_mfma_f32_16x16x32_bf16 v[0:3], a[104:107], a[72:75], v[0:3]\n\t"
    "v_mfma_f32_16x16x32_bf16 v[0:3], a[108:111], a[76:79], v[0:3]\n\t"
    "v_mfma_f32_16x16x32_bf16 v[4:7], a[112:115], a[64:67], v[40:43]\n\t"
    "v_mfma_f32_16x16x32_bf16 v[4:7], a[116:119], a[68:71], v[4:7]\n\t"
    "v_mfma_f32_16x16x32_bf16 v[4:7], a[120:123], a[72:75], v[4:7]\n\t"
    "v_mfma_f32_16x16x32_bf16 v[4:7], a[124:127], a[76:79], v[4:7]\n\t"
    "v_mfma_f32_16x16x32_bf16 v[8:11], a[96:99], a[80:83], v[44:47]\n\t"
    "ds_read_b128 a[96:99], v48 offset:40960\n\t"
    "v_mfma_f32_16x16x32_bf16 v[8:11], a[100:103], a[84:87], v[8:11]\n\t"
    "ds_read_b128 a[100:103], v49 offset:40960\n\t"
    "v_mfma_f32_16x16x32_bf16 v[8:11], a[104:107], a[88:91], v[8:11]\n\t"
    "ds_read_b128 a[104:107], v48 offset:41984\n\t"
    "v_mfma_f32_16x16x32_bf16 v[8:11], a[108:111], a[92:95], v[8:11]\n\t"
    "ds_read_b128 a[108:111], v49 offset:41984\n\t"
    "v_mfma_f32_16x16x32_bf16 v[12:15], a[112:115], a[80:83], v[44:47]\n\t"
    "ds_read_b128 a[112:115], v48 offset:45056\n\t"
    "v_mfma_f32_16x16x32_bf16 v[12:15], a[116:119], a[84:87], v[12:15]\n\t"
    "ds_read_b128 a[116:119], v49 offset:45056\n\t"
    "v_mfma_f32_16x16x32_bf16 v[12:15], a[120:123], a[88:91], v[12:15]\n\t"
    "ds_read_b128 a[120:123], v48 offset:46080\n\t"
    "v_mfma_f32_16x16x32_bf16 v[12:15], a[124:127], a[92:95], v[12:15]\n\t"
    "ds_read_b128 a[124:127], v49 offset:46080\n\t"
    "v_mfma_f32_16x16x32_bf16 v[128:131], a[128:131], v[16:19], v[128:131]\n\t"
    "v_exp_f32 v0, v0\n\t"
    "v_mfma_f32_16x16x32_bf16 v[132:135], a[132:135], v[16:19], v[132:135]\n\t"
    "v_exp_f32 v1, v1\n\t"
    "v_mfma_f32_16x16x32_bf16 v[136:139], a[136:139], v[16:19], v[136:139]\n\t"
    "v_exp_f32 v2, v2\n\t"
    "v_mfma_f32_16x16x32_bf16 v[140:143], a[140:143], v[16:19], v[140:143]\n\t"
    "v_exp_f32 v3, v3\n\t"
    "v_mfma_f32_16x16x32_bf16 v[144:147], a[144:147], v[16:19], v[144:147]\n\t"
    "v_exp_f32 v4, v4\n\t"
    "v_mfma_f32_16x16x32_bf16 v[148:151], a[148:151], v[16:19], v[148:151]\n\t"
    "v_exp_f32 v5, v5\n\t"
    "v_mfma_f32_16x16x32_bf16 v[152:155], a[152:155], v[16:19], v[152:155]\n\t"
    "v_exp_f32 v6, v6\n\t"
    "v_mfma_f32_16x16x32_bf16 v[156:159], a[156:159], v[16:19], v[156:159]\n\t"
    "v_exp_f32 v7, v7\n\t"
    "v_mfma_f32_16x16x32_bf16 a[168:171], a[184:187], v[16:19], a[168:171]\n\t"
    "v_cvt_pk_bf16_f32 v16, v0, v1\n\t"
    "v_cvt_pk_bf16_f32 v17, v2, v3\n\t"
    "v_mfma_f32_16x16x32_bf16 v[160:163], a[128:131], v[20:23], v[160:163]\n\t"
    "v_cvt_pk_bf16_f32 v18, v4, v5\n\t"
    "v_cvt_pk_bf16_f32 v19, v6, v7\n\t"
    "v_mfma_f32_16x16x32_bf16 v[164:167], a[132:135], v[20:23], v[164:167]\n\t"
    "v_exp_f32 v8, v8\n\t"
    "v_mfma_f32_16x16x32_bf16 v[168:171], a[136:139], v[20:23], v[168:171]\n\t"
    "v_exp_f32 v9, v9\n\t"
    "v_mfma_f32_16x16x32_bf16 v[172:175], a[140:143], v[20:23], v[172:175]\n\t"
    "v_exp_f32 v10, v10\n\t"
    "v_mfma_f32_16x16x32_bf16 v[176:179], a[144:147], v[20:23], v[176:179]\n\t"
    "v_exp_f32 v11, v11\n\t"
    "v_mfma_f32_16x16x32_bf16 v[180:183], a[148:151], v[20:23], v[180:183]\n\t"
    "v_exp_f32 v12, v12\n\t"
    "v_mfma_f32_16x16x32_bf16 v[184:187], a[152:155], v[20:23], v[184:187]\n\t"
    "v_exp_f32 v13, v13\n\t"
    "v_mfma_f32_16x16x32_bf16 v[188:191], a[156:159], v[20:23], v[188:191]\n\t"
    "v_exp_f32 v14, v14\n\t"
    "v_mfma_f32_16x16x32_bf16 a[172:175], a[184:187], v[20:23], a[172:175]\n\t"
    "v_cvt_pk_bf16_f32 v20, v8, v9\n\t"
    "v_cvt_pk_bf16_f32 v21, v10, v11\n\t"
    "s_waitcnt lgkmcnt(0)\n\t"
    "v_mfma_f32_16x16x32_bf16 v[0:3], a[96:99], a[0:3], v[24:27]\n\t"
    "v_exp_f32 v15, v15\n\t"
    "v_mfma_f32_16x16x32_bf16 v[0:3], a[100:103], a[4:7], v[0:3]\n\t"
    "v_cvt_pk_bf16_f32 v22, v12, v13\n\t"
    "v_cvt_pk_bf16_f32 v23, v14, v15\n\t"
    "v_mfma_f32_16x16x32_bf16 v[0:3], a[104:107], a[8:11], v[0:3]\n\t"
    "v_mfma_f32_16x16x32_bf16 v[0:3], a[108:111], a[12:15], v[0:3]\n\t"
    "v_mfma_f32_16x16x32_bf16 v[4:7], a[112:115], a[0:3], v[24:27]\n\t"
    "v_mfma_f32_16x16x32_bf16 v[4:7], a[116:119], a[4:7], v[4:7]\n\t"
    "v_mfma_f32_16x16x32_bf16 v[4:7], a[120:123], a[8:11], v[4:7]\n\t"
    "v_mfma_f32_16x16x32_bf16 v[4:7], a[124:127], a[12:15], v[4:7]\n\t"
    "v_mfma_f32_16x16x32_bf16 v[8:11], a[96:99], a[16:19], v[28:31]\n\t"
    "v_mfma_f32_16x16x32_bf16 v[8:11], a[100:103], a[20:23], v[8:11]\n\t"
    "v_mfma_f32_16x16x32_bf16 v[8:11], a[104:107], a[24:27], v[8:11]\n\t"
    "v_mfma_f32_16x16x32_bf16 v[8:11], a[108:111], a[28:31], v[8:11]\n\t"
    "v_exp_f32 v0, v0\n\t"
    "v_mfma_f32_16x16x32_bf16 v[12:15], a[112:115], a[16:19], v[28:31]\n\t"
    "v_exp_f32 v1, v1\n\t"
    "v_mfma_f32_16x16x32_bf16 v[12:15], a[116:119], a[20:23], v[12:15]\n\t"
    "v_exp_f32 v2, v2\n\t"
    "v_mfma_f32_16x16x32_bf16 v[12:15], a[120:123], a[24:27], v[12:15]\n\t"
    "v_exp_f32 v3, v3\n\t"
    "v_mfma_f32_16x16x32_bf16 v[12:15], a[124:127], a[28:31], v[12:15]\n\t"
    "v_exp_f32 v4, v4\n\t"
    "v_mfma_f32_16x16x32_bf16 v[192:195], a[128:131], v[16:19], v[192:195]\n\t"
    "v_exp_f32 v5, v5\n\t"
    "v_mfma_f32_16x16x32_bf16 v[196:199], a[132:135], v[16:19], v[196:199]\n\t"
    "v_exp_f32 v6, v6\n\t"
    "v_mfma_f32_16x16x32_bf16 v[200:203], a[136:139], v[16:19], v[200:203]\n\t"
    "v_exp_f32 v7, v7\n\t"
    "v_mfma_f32_16x16x32_bf16 v[204:207], a[140:143], v[16:19], v[204:207]\n\t"
    "v_exp_f32 v8, v8\n\t"
    "v_mfma_f32_16x16x32_bf16 v[208:211], a[144:147], v[16:19], v[208:211]\n\t"
    "v_exp_f32 v9, v9\n\t"
    "v_mfma_f32_16x16x32_bf16 v[212:215], a[148:151], v[16:19], v[212:215]\n\t"
    "v_exp_f32 v10, v10\n\t"
    "v_mfma_f32_16x16x32_bf16 v[216:219], a[152:155], v[16:19], v[216:219]\n\t"
    "v_exp_f32 v11, v11\n\t"
    "v_mfma_f32_16x16x32_bf16 v[220:223], a[156:159], v[16:19], v[220:223]\n\t"
    "v_exp_f32 v12, v12\n\t"
    "v_mfma_f32_16x16x32_bf16 a[176:179], a[184:187], v[16:19], a[176:179]\n\t"
    "v_cvt_pk_bf16_f32 v16, v0, v1\n\t"
    "v_cvt_pk_bf16_f32 v17, v2, v3\n\t"
    "v_mfma_f32_16x16x32_bf16 v[224:227], a[128:131], v[20:23], v[224:227]\n\t"
    "ds_read_b64_tr_b16 a[128:129], v50 offset:40960\n\t"
    "v_cvt_pk_bf16_f32 v18, v4, v5\n\t"
    "v_mfma_f32_16x16x32_bf16 v[228:231], a[132:135], v[20:23], v[228:231]\n\t"
    "ds_read_b64_tr_b16 a[130:131], v50 offset:45056\n\t"
    "v_cvt_pk_bf16_f32 v19, v6, v7\n\t"
    "v_mfma_f32_16x16x32_bf16 v[232:235], a[136:139], v[20:23], v[232:235]\n\t"
    "ds_read_b64_tr_b16 a[132:133], v50 offset:41216\n\t"
    "ds_read_b64_tr_b16 a[134:135], v50 offset:45312\n\t"
    "v_mfma_f32_16x16x32_bf16 v[236:239], a[140:143], v[20:23], v[236:239]\n\t"
    "ds_read_b64_tr_b16 a[136:137], v50 offset:41472\n\t"
    "ds_read_b64_tr_b16 a[138:139], v50 offset:45568\n\t"
    "v_mfma_f32_16x16x32_bf16 v[240:243], a[144:147], v[20:23], v[240:243]\n\t"
    "ds_read_b64_tr_b16 a[140:141], v50 offset:41728\n\t"
    "ds_read_b64_tr_b16 a[142:143], v50 offset:45824\n\t"
    "v_mfma_f32_16x16x32_bf16 v[244:247], a[148:151], v[20:23], v[244:247]\n\t"
    "v_exp_f32 v13, v13\n\t"
    "v_mfma_f32_16x16x32_bf16 v[248:251], a[152:155], v[20:23], v[248:251]\n\t"
    "v_exp_f32 v14, v14\n\t"
    "v_mfma_f32_16x16x32_bf16 v[252:255], a[156:159], v[20:23], v[252:255]\n\t"
    "v_exp_f32 v15, v15\n\t"
    "v_mfma_f32_16x16x32_bf16 a[180:183], a[184:187], v[20:23], a[180:183]\n\t"
    "ds_read_b64_tr_b16 a[144:145], v50 offset:41984\n\t"
    "v_cvt_pk_bf16_f32 v20, v8, v9\n\t"
    "v_mfma_f32_16x16x32_bf16 v[0:3], a[96:99], a[32:35], v[32:35]\n\t"
    "ds_read_b64_tr_b16 a[146:147], v50 offset:46080\n\t"
    "v_cvt_pk_bf16_f32 v21, v10, v11\n\t"
    "v_mfma_f32_16x16x32_bf16 v[0:3], a[100:103], a[36:39], v[0:3]\n\t"
    "ds_read_b64_tr_b16 a[148:149], v50 offset:42240\n\t"
    "v_cvt_pk_bf16_f32 v22, v12, v13\n\t"
    "v_mfma_f32_16x16x32_bf16 v[0:3], a[104:107], a[40:43], v[0:3]\n\t"
    "ds_read_b64_tr_b16 a[150:151], v50 offset:46336\n\t"
    "v_cvt_pk_bf16_f32 v23, v14, v15\n\t"
    "v_mfma_f32_16x16x32_bf16 v[0:3], a[108:111], a[44:47], v[0:3]\n\t"
    "ds_read_b64_tr_b16 a[152:153], v50 offset:42496\n\t"
    "ds_read_b64_tr_b16 a[154:155], v50 offset:46592\n\t"
    "v_mfma_f32_16x16x32_bf16 v[4:7], a[112:115], a[32:35], v[32:35]\n\t"
    "ds_read_b64_tr_b16 a[156:157], v50 offset:42752\n\t"
    "ds_read_b64_tr_b16 a[158:159], v50 offset:46848\n\t"
    "v_mfma_f32_16x16x32_bf16 v[4:7], a[116:119], a[36:39], v[4:7]\n\t"
    "v_mfma_f32_16x16x32_bf16 v[4:7], a[120:123], a[40:43], v[4:7]\n\t"
    "v_mfma_f32_16x16x32_bf16 v[4:7], a[124:127], a[44:47], v[4:7]\n\t"
    "v_exp_f32 v0, v0\n\t"
    "v_mfma_f32_16x16x32_bf16 v[8:11], a[96:99], a[48:51], v[36:39]\n\t"
    "v_exp_f32 v1, v1\n\t"
    "v_mfma_f32_16x16x32_bf16 v[8:11], a[100:103], a[52:55], v[8:11]\n\t"
    "v_exp_f32 v2, v2\n\t"
    "v_mfma_f32_16x16x32_bf16 v[8:11], a[104:107], a[56:59], v[8:11]\n\t"
    "v_exp_f32 v3, v3\n\t"
    "v_mfma_f32_16x16x32_bf16 v[8:11], a[108:111], a[60:63], v[8:11]\n\t"
    "v_exp_f32 v4, v4\n\t"
    "v_mfma_f32_16x16x32_bf16 v[12:15], a[112:115], a[48:51], v[36:39]\n\t"
    "v_exp_f32 v5, v5\n\t"
    "v_mfma_f32_16x16x32_bf16 v[12:15], a[116:119], a[52:55], v[12:15]\n\t"
    "v_exp_f32 v6, v6\n\t"
    "v_mfma_f32_16x16x32_bf16 v[12:15], a[120:123], a[56:59], v[12:15]\n\t"
    "v_exp_f32 v7, v7\n\t"
    "v_mfma_f32_16x16x32_bf16 v[12:15], a[124:127], a[60:63], v[12:15]\n\t"
    "v_exp_f32 v8, v8\n\t"
    "s_waitcnt lgkmcnt(0)\n\t"
    "v_mfma_f32_16x16x32_bf16 v[64:67], a[128:131], v[16:19], v[64:67]\n\t"
    "v_exp_f32 v9, v9\n\t"
    "v_mfma_f32_16x16x32_bf16 v[68:71], a[132:135], v[16:19], v[68:71]\n\t"
    "v_exp_f32 v10, v10\n\t"
    "v_mfma_f32_16x16x32_bf16 v[72:75], a[136:139], v[16:19], v[72:75]\n\t"
    "v_exp_f32 v11, v11\n\t"
    "v_mfma_f32_16x16x32_bf16 v[76:79], a[140:143], v[16:19], v[76:79]\n\t"
    "v_exp_f32 v12, v12\n\t"
    "v_mfma_f32_16x16x32_bf16 v[80:83], a[144:147], v[16:19], v[80:83]\n\t"
    "v_exp_f32 v13, v13\n\t"
    "v_mfma_f32_16x16x32_bf16 v[84:87], a[148:151], v[16:19], v[84:87]\n\t"
    "v_exp_f32 v14, v14\n\t"
    "v_mfma_f32_16x16x32_bf16 v[88:91], a[152:155], v[16:19], v[88:91]\n\t"
    "v_exp_f32 v15, v15\n\t"
    "v_mfma_f32_16x16x32_bf16 v[92:95], a[156:159], v[16:19], v[92:95]\n\t"
    "v_mfma_f32_16x16x32_bf16 a[160:163], a[184:187], v[16:19], a[160:163]\n\t"
    "v_cvt_pk_bf16_f32 v16, v0, v1\n\t"
    "v_cvt_pk_bf16_f32 v17, v2, v3\n\t"
    "v_mfma_f32_16x16x32_bf16 v[96:99], a[128:131], v[20:23], v[96:99]\n\t"
    "v_cvt_pk_bf16_f32 v18, v4, v5\n\t"
    "v_cvt_pk_bf16_f32 v19, v6, v7\n\t"
    "v_mfma_f32_16x16x32_bf16 v[100:103], a[132:135], v[20:23], v[100:103]\n\t"
    "v_mfma_f32_16x16x32_bf16 v[104:107], a[136:139], v[20:23], v[104:107]\n\t"
    "v_mfma_f32_16x16x32_bf16 v[108:111], a[140:143], v[20:23], v[108:111]\n\t"
    "v_mfma_f32_16x16x32_bf16 v[112:115], a[144:147], v[20:23], v[112:115]\n\t"
    "v_mfma_f32_16x16x32_bf16 v[116:119], a[148:151], v[20:23], v[116:119]\n\t"
    "v_mfma_f32_16x16x32_bf16 v[120:123], a[152:155], v[20:23], v[120:123]\n\t"
    "v_mfma_f32_16x16x32_bf16 v[124:127], a[156:159], v[20:23], v[124:127]\n\t"
    "v_mfma_f32_16x16x32_bf16 a[164:167], a[184:187], v[20:23], a[164:167]\n\t"
    "v_cvt_pk_bf16_f32 v20, v8, v9\n\t"
    "v_cvt_pk_bf16_f32 v21, v10, v11\n\t"
    "v_mfma_f32_16x16x32_bf16 v[0:3], a[96:99], a[64:67], v[40:43]\n\t"
    "v_cvt_pk_bf16_f32 v22, v12, v13\n\t"
    "v_cvt_pk_bf16_f32 v23, v14, v15\n\t"
    "v_mfma_f32_16x16x32_bf16 v[0:3], a[100:103], a[68:71], v[0:3]\n\t"
    "v_mfma_f32_16x16x32_bf16 v[0:3], a[104:107], a[72:75], v[0:3]\n\t"
    "v_mfma_f32_16x16x32_bf16 v[0:3], a[108:111], a[76:79], v[0:3]\n\t"
    "v_mfma_f32_16x16x32_bf16 v[4:7], a[112:115], a[64:67], v[40:43]\n\t"
    "v_mfma_f32_16x16x32_bf16 v[4:7], a[116:119], a[68:71], v[4:7]\n\t"
    "v_mfma_f32_16x16x32_bf16 v[4:7], a[120:123], a[72:75], v[4:7]\n\t"
    "v_mfma_f32_16x16x32_bf16 v[4:7], a[124:127], a[76:79], v[4:7]\n\t"
    "v_mfma_f32_16x16x32_bf16 v[8:11], a[96:99], a[80:83], v[44:47]\n\t"
    "v_mfma_f32_16x16x32_bf16 v[8:11], a[100:103], a[84:87], v[8:11]\n\t"
    "v_mfma_f32_16x16x32_bf16 v[8:11], a[104:107], a[88:91], v[8:11]\n\t"
    "v_mfma_f32_16x16x32_bf16 v[8:11], a[108:111], a[92:95], v[8:11]\n\t"
    "v_exp_f32 v0, v0\n\t"
    "v_mfma_f32_16x16x32_bf16 v[12:15], a[112:115], a[80:83], v[44:47]\n\t"
    "v_exp_f32 v1, v1\n\t"
    "v_mfma_f32_16x16x32_bf16 v[12:15], a[116:119], a[84:87], v[12:15]\n\t"
    "v_exp_f32 v2, v2\n\t"
    "v_mfma_f32_16x16x32_bf16 v[12:15], a[120:123], a[88:91], v[12:15]\n\t"
    "v_exp_f32 v3, v3\n\t"
    "v_mfma_f32_16x16x32_bf16 v[12:15], a[124:127], a[92:95], v[12:15]\n\t"
    "s_waitcnt vmcnt(8)\n\t"
    "s_barrier\n\t"
    "v_mfma_f32_16x16x32_bf16 v[128:131], a[128:131], v[16:19], v[128:131]\n\t"
    "ds_read_b128 a[96:99], v48 offset:49152\n\t"
    "ds_read_b128 a[100:103], v49 offset:49152\n\t"
    "v_mfma_f32_16x16x32_bf16 v[132:135], a[132:135], v[16:19], v[132:135]\n\t"
    "ds_read_b128 a[104:107], v48 offset:50176\n\t"
    "ds_read_b128 a[108:111], v49 offset:50176\n\t"
    "v_mfma_f32_16x16x32_bf16 v[136:139], a[136:139], v[16:19], v[136:139]\n\t"
    "ds_read_b128 a[112:115], v48 offset:53248\n\t"
    "ds_read_b128 a[116:119], v49 offset:53248\n\t"
    "v_mfma_f32_16x16x32_bf16 v[140:143], a[140:143], v[16:19], v[140:143]\n\t"
    "ds_read_b128 a[120:123], v48 offset:54272\n\t"
    "ds_read_b128 a[124:127], v49 offset:54272\n\t"
    "v_mfma_f32_16x16x32_bf16 v[144:147], a[144:147], v[16:19], v[144:147]\n\t"
    "v_exp_f32 v4, v4\n\t"
    "v_mfma_f32_16x16x32_bf16 v[148:151], a[148:151], v[16:19], v[148:151]\n\t"
    "v_exp_f32 v5, v5\n\t"
    "v_mfma_f32_16x16x32_bf16 v[152:155], a[152:155], v[16:19], v[152:155]\n\t"
    "v_exp_f32 v6, v6\n\t"
    "v_mfma_f32_16x16x32_bf16 v[156:159], a[156:159], v[16:19], v[156:159]\n\t"
    "v_exp_f32 v7, v7\n\t"
    "s_add_u32 m0, s53, 16384\n\t"
    "s_add_u32 s58, s52, 81920\n\t"
    "v_mfma_f32_16x16x32_bf16 a[168:171], a[184:187], v[16:19], a[168:171]\n\t"
    "v_cvt_pk_bf16_f32 v16, v0, v1\n\t"
    "v_cvt_pk_bf16_f32 v17, v2, v3\n\t"
    "buffer_load_dwordx4 v51, s[40:43], s58 offen nt lds\n\t"
    "v_mfma_f32_16x16x32_bf16 v[160:163], a[128:131], v[20:23], v[160:163]\n\t"
    "v_cvt_pk_bf16_f32 v18, v4, v5\n\t"
    "v_cvt_pk_bf16_f32 v19, v6, v7\n\t"
    "v_mfma_f32_16x16x32_bf16 v[164:167], a[132:135], v[20:23], v[164:167]\n\t"
    "v_exp_f32 v8, v8\n\t"
    "v_mfma_f32_16x16x32_bf16 v[168:171], a[136:139], v[20:23], v[168:171]\n\t"
    "v_exp_f32 v9, v9\n\t"
    "v_mfma_f32_16x16x32_bf16 v[172:175], a[140:143], v[20:23], v[172:175]\n\t"
    "v_exp_f32 v10, v10\n\t"
    "v_mfma_f32_16x16x32_bf16 v[176:179], a[144:147], v[20:23], v[176:179]\n\t"
    "v_exp_f32 v11, v11\n\t"
    "v_mfma_f32_16x16x32_bf16 v[180:183], a[148:151], v[20:23], v[180:183]\n\t"
    "v_exp_f32 v12, v12\n\t"
    "v_mfma_f32_16x16x32_bf16 v[184:187], a[152:155], v[20:23], v[184:187]\n\t"
    "v_exp_f32 v13, v13\n\t"
    "v_mfma_f32_16x16x32_bf16 v[188:191], a[156:159], v[20:23], v[188:191]\n\t"
    "v_exp_f32 v14, v14\n\t"
    "v_mfma_f32_16x16x32_bf16 a[172:175], a[184:187], v[20:23], a[172:175]\n\t"
    "v_cvt_pk_bf16_f32 v20, v8, v9\n\t"
    "v_cvt_pk_bf16_f32 v21, v10, v11\n\t"
    "buffer_load_dwordx4 v52, s[40:43], s58 offen offset:2048 nt lds\n\t"
    "s_waitcnt lgkmcnt(0)\n\t"
    "v_mfma_f32_16x16x32_bf16 v[0:3], a[96:99], a[0:3], v[24:27]\n\t"
    "v_exp_f32 v15, v15\n\t"
    "v_mfma_f32_16x16x32_bf16 v[0:3], a[100:103], a[4:7], v[0:3]\n\t"
    "v_cvt_pk_bf16_f32 v22, v12, v13\n\t"
    "v_cvt_pk_bf16_f32 v23, v14, v15\n\t"
    "v_mfma_f32_16x16x32_bf16 v[0:3], a[104:107], a[8:11], v[0:3]\n\t"
    "s_add_u32 m0, s54, 16384\n\t"
    "v_mfma_f32_16x16x32_bf16 v[0:3], a[108:111], a[12:15], v[0:3]\n\t"
    "buffer_load_dwordx4 v53, s[44:47], s58 offen nt lds\n\t"
    "v_mfma_f32_16x16x32_bf16 v[4:7], a[112:115], a[0:3], v[24:27]\n\t"
    "v_mfma_f32_16x16x32_bf16 v[4:7], a[116:119], a[4:7], v[4:7]\n\t"
    "v_mfma_f32_16x16x32_bf16 v[4:7], a[120:123], a[8:11], v[4:7]\n\t"
    "v_mfma_f32_16x16x32_bf16 v[4:7], a[124:127], a[12:15], v[4:7]\n\t"
    "v_mfma_f32_16x16x32_bf16 v[8:11], a[96:99], a[16:19], v[28:31]\n\t"
    "buffer_load_dwordx4 v53, s[44:47], s58 offen offset:2048 nt lds\n\t"
    "v_mfma_f32_16x16x32_bf16 v[8:11], a[100:103], a[20:23], v[8:11]\n\t"
    "v_exp_f32 v0, v0\n\t"
    "v_mfma_f32_16x16x32_bf16 v[8:11], a[104:107], a[24:27], v[8:11]\n\t"
    "v_exp_f32 v1, v1\n\t"
    "v_mfma_f32_16x16x32_bf16 v[8:11], a[108:111], a[28:31], v[8:11]\n\t"
    "v_exp_f32 v2, v2\n\t"
    "v_mfma_f32_16x16x32_bf16 v[12:15], a[112:115], a[16:19], v[28:31]\n\t"
    "v_exp_f32 v3, v3\n\t"
    "v_mfma_f32_16x16x32_bf16 v[12:15], a[116:119], a[20:23], v[12:15]\n\t"
    "v_exp_f32 v4, v4\n\t"
    "v_mfma_f32_16x16x32_bf16 v[12:15], a[120:123], a[24:27], v[12:15]\n\t"
    "v_exp_f32 v5, v5\n\t"
    "v_mfma_f32_16x16x32_bf16 v[12:15], a[124:127], a[28:31], v[12:15]\n\t"
    "v_exp_f32 v6, v6\n\t"
    "v_mfma_f32_16x16x32_bf16 v[192:195], a[128:131], v[16:19], v[192:195]\n\t"
    "v_exp_f32 v7, v7\n\t"
    "v_mfma_f32_16x16x32_bf16 v[196:199], a[132:135], v[16:19], v[196:199]\n\t"
    "v_exp_f32 v8, v8\n\t"
    "v_mfma_f32_16x16x32_bf16 v[200:203], a[136:139], v[16:19], v[200:203]\n\t"
    "v_exp_f32 v9, v9\n\t"
    "v_mfma_f32_16x16x32_bf16 v[204:207], a[140:143], v[16:19], v[204:207]\n\t"
    "v_exp_f32 v10, v10\n\t"
    "v_mfma_f32_16x16x32_bf16 v[208:211], a[144:147], v[16:19], v[208:211]\n\t"
    "v_exp_f32 v11, v11\n\t"
    "v_mfma_f32_16x16x32_bf16 v[212:215], a[148:151], v[16:19], v[212:215]\n\t"
    "v_exp_f32 v12, v12\n\t"
    "v_mfma_f32_16x16x32_bf16 v[216:219], a[152:155], v[16:19], v[216:219]\n\t"
    "v_exp_f32 v13, v13\n\t"
    "v_mfma_f32_16x16x32_bf16 v[220:223], a[156:159], v[16:19], v[220:223]\n\t"
    "v_exp_f32 v14, v14\n\t"
    "v_mfma_f32_16x16x32_bf16 a[176:179], a[184:187], v[16:19], a[176:179]\n\t"
    "v_cvt_pk_bf16_f32 v16, v0, v1\n\t"
    "v_cvt_pk_bf16_f32 v17, v2, v3\n\t"
    "v_mfma_f32_16x16x32_bf16 v[224:227], a[128:131], v[20:23], v[224:227]\n\t"
    "ds_read_b64_tr_b16 a[128:129], v50 offset:49152\n\t"
    "v_cvt_pk_bf16_f32 v18, v4, v5\n\t"
    "s_add_u32 m0, s53, 24576\n\t"
    "s_add_u32 s58, s52, 90112\n\t"
    "v_mfma_f32_16x16x32_bf16 v[228:231], a[132:135], v[20:23], v[228:231]\n\t"
    "ds_read_b64_tr_b16 a[130:131], v50 offset:53248\n\t"
    "v_cvt_pk_bf16_f32 v19, v6, v7\n\t"
    "buffer_load_dwordx4 v51, s[40:43], s58 offen nt lds\n\t"
    "v_mfma_f32_16x16x32_bf16 v[232:235], a[136:139], v[20:23], v[232:235]\n\t"
    "ds_read_b64_tr_b16 a[132:133], v50 offset:49408\n\t"
    "ds_read_b64_tr_b16 a[134:135], v50 offset:53504\n\t"
    "v_mfma_f32_16x16x32_bf16 v[236:239], a[140:143], v[20:23], v[236:239]\n\t"
    "ds_read_b64_tr_b16 a[136:137], v50 offset:49664\n\t"
    "ds_read_b64_tr_b16 a[138:139], v50 offset:53760\n\t"
    "v_mfma_f32_16x16x32_bf16 v[240:243], a[144:147], v[20:23], v[240:243]\n\t"
    "ds_read_b64_tr_b16 a[140:141], v50 offset:49920\n\t"
    "ds_read_b64_tr_b16 a[142:143], v50 offset:54016\n\t"
    "v_mfma_f32_16x16x32_bf16 v[244:247], a[148:151], v[20:23], v[244:247]\n\t"
    "v_exp_f32 v15, v15\n\t"
    "v_mfma_f32_16x16x32_bf16 v[248:251], a[152:155], v[20:23], v[248:251]\n\t"
    "ds_read_b64_tr_b16 a[144:145], v50 offset:50176\n\t"
    "ds_read_b64_tr_b16 a[146:147], v50 offset:54272\n\t"
    "v_mfma_f32_16x16x32_bf16 v[252:255], a[156:159], v[20:23], v[252:255]\n\t"
    "ds_read_b64_tr_b16 a[148:149], v50 offset:50432\n\t"
    "ds_read_b64_tr_b16 a[150:151], v50 offset:54528\n\t"
    "v_mfma_f32_16x16x32_bf16 a[180:183], a[184:187], v[20:23], a[180:183]\n\t"
    "ds_read_b64_tr_b16 a[152:153], v50 offset:50688\n\t"
    "v_cvt_pk_bf16_f32 v20, v8, v9\n\t"
    "v_mfma_f32_16x16x32_bf16 v[0:3], a[96:99], a[32:35], v[32:35]\n\t"
    "ds_read_b64_tr_b16 a[154:155], v50 offset:54784\n\t"
    "v_cvt_pk_bf16_f32 v21, v10, v11\n\t"
    "v_mfma_f32_16x16x32_bf16 v[0:3], a[100:103], a[36:39], v[0:3]\n\t"
    "ds_read_b64_tr_b16 a[156:157], v50 offset:50944\n\t"
    "v_cvt_pk_bf16_f32 v22, v12, v13\n\t"
    "buffer_load_dwordx4 v52, s[40:43], s58 offen offset:2048 nt lds\n\t"
    "v_mfma_f32_16x16x32_bf16 v[0:3], a[104:107], a[40:43], v[0:3]\n\t"
    "ds_read_b64_tr_b16 a[158:159], v50 offset:55040\n\t"
    "v_cvt_pk_bf16_f32 v23, v14, v15\n\t"
    "v_mfma_f32_16x16x32_bf16 v[0:3], a[108:111], a[44:47], v[0:3]\n\t"
    "s_add_u32 m0, s54, 24576\n\t"
    "v_mfma_f32_16x16x32_bf16 v[4:7], a[112:115], a[32:35], v[32:35]\n\t"
    "buffer_load_dwordx4 v53, s[44:47], s58 offen nt lds\n\t"
    "v_mfma_f32_16x16x32_bf16 v[4:7], a[116:119], a[36:39], v[4:7]\n\t"
    "v_mfma_f32_16x16x32_bf16 v[4:7], a[120:123], a[40:43], v[4:7]\n\t"
    "v_mfma_f32_16x16x32_bf16 v[4:7], a[124:127], a[44:47], v[4:7]\n\t"
    "v_mfma_f32_16x16x32_bf16 v[8:11], a[96:99], a[48:51], v[36:39]\n\t"
    "v_mfma_f32_16x16x32_bf16 v[8:11], a[100:103], a[52:55], v[8:11]\n\t"
    "v_exp_f32 v0, v0\n\t"
    "v_mfma_f32_16x16x32_bf16 v[8:11], a[104:107], a[56:59], v[8:11]\n\t"
    "v_exp_f32 v1, v1\n\t"
    "v_mfma_f32_16x16x32_bf16 v[8:11], a[108:111], a[60:63], v[8:11]\n\t"
    "v_exp_f32 v2, v2\n\t"
    "v_mfma_f32_16x16x32_bf16 v[12:15], a[112:115], a[48:51], v[36:39]\n\t"
    "v_exp_f32 v3, v3\n\t"
    "v_mfma_f32_16x16x32_bf16 v[12:15], a[116:119], a[52:55], v[12:15]\n\t"
    "v_exp_f32 v4, v4\n\t"
    "v_mfma_f32_16x16x32_bf16 v[12:15], a[120:123], a[56:59], v[12:15]\n\t"
    "v_exp_f32 v5, v5\n\t"
    "v_mfma_f32_16x16x32_bf16 v[12:15], a[124:127], a[60:63], v[12:15]\n\t"
    "v_exp_f32 v6, v6\n\t"
    "s_waitcnt lgkmcnt(0)\n\t"
    "v_mfma_f32_16x16x32_bf16 v[64:67], a[128:131], v[16:19], v[64:67]\n\t"
    "v_exp_f32 v7, v7\n\t"
    "v_mfma_f32_16x16x32_bf16 v[68:71], a[132:135], v[16:19], v[68:71]\n\t"
    "v_exp_f32 v8, v8\n\t"
    "v_mfma_f32_16x16x32_bf16 v[72:75], a[136:139], v[16:19], v[72:75]\n\t"
    "v_exp_f32 v9, v9\n\t"
    "v_mfma_f32_16x16x32_bf16 v[76:79], a[140:143], v[16:19], v[76:79]\n\t"
    "v_exp_f32 v10, v10\n\t"
    "buffer_load_dwordx4 v53, s[44:47], s58 offen offset:2048 nt lds\n\t"
    "v_mfma_f32_16x16x32_bf16 v[80:83], a[144:147], v[16:19], v[80:83]\n\t"
    "v_exp_f32 v11, v11\n\t"
    "v_mfma_f32_16x16x32_bf16 v[84:87], a[148:151], v[16:19], v[84:87]\n\t"
    "v_exp_f32 v12, v12\n\t"
    "v_mfma_f32_16x16x32_bf16 v[88:91], a[152:155], v[16:19], v[88:91]\n\t"
    "v_exp_f32 v13, v13\n\t"
    "v_mfma_f32_16x16x32_bf16 v[92:95], a[156:159], v[16:19], v[92:95]\n\t"
    "v_exp_f32 v14, v14\n\t"
    "v_mfma_f32_16x16x32_bf16 a[160:163], a[184:187], v[16:19], a[160:163]\n\t"
    "v_cvt_pk_bf16_f32 v16, v0, v1\n\t"
    "v_cvt_pk_bf16_f32 v17, v2, v3\n\t"
    "v_mfma_f32_16x16x32_bf16 v[96:99], a[128:131], v[20:23], v[96:99]\n\t"
    "v_cvt_pk_bf16_f32 v18, v4, v5\n\t"
    "v_cvt_pk_bf16_f32 v19, v6, v7\n\t"
    "v_mfma_f32_16x16x32_bf16 v[100:103], a[132:135], v[20:23], v[100:103]\n\t"
    "v_exp_f32 v15, v15\n\t"
    "v_mfma_f32_16x16x32_bf16 v[104:107], a[136:139], v[20:23], v[104:107]\n\t"
    "v_mfma_f32_16x16x32_bf16 v[108:111], a[140:143], v[20:23], v[108:111]\n\t"
    "v_mfma_f32_16x16x32_bf16 v[112:115], a[144:147], v[20:23], v[112:115]\n\t"
    "v_mfma_f32_16x16x32_bf16 v[116:119], a[148:151], v[20:23], v[116:119]\n\t"
    "v_mfma_f32_16x16x32_bf16 v[120:123], a[152:155], v[20:23], v[120:123]\n\t"
    "v_mfma_f32_16x16x32_bf16 v[124:127], a[156:159], v[20:23], v[124:127]\n\t"
    "v_mfma_f32_16x16x32_bf16 a[164:167], a[184:187], v[20:23], a[164:167]\n\t"
    "v_cvt_pk_bf16_f32 v20, v8, v9\n\t"
    "v_cvt_pk_bf16_f32 v21, v10, v11\n\t"
    "v_mfma_f32_16x16x32_bf16 v[0:3], a[96:99], a[64:67], v[40:43]\n\t"
    "v_cvt_pk_bf16_f32 v22, v12, v13\n\t"
    "v_cvt_pk_bf16_f32 v23, v14, v15\n\t"
    "v_mfma_f32_16x16x32_bf16 v[0:3], a[100:103], a[68:71], v[0:3]\n\t"
    "v_mfma_f32_16x16x32_bf16 v[0:3], a[104:107], a[72:75], v[0:3]\n\t"
    "v_mfma_f32_16x16x32_bf16 v[0:3], a[108:111], a[76:79], v[0:3]\n\t"
    "v_mfma_f32_16x16x32_bf16 v[4:7], a[112:115], a[64:67], v[40:43]\n\t"
    "v_mfma_f32_16x16x32_bf16 v[4:7], a[116:119], a[68:71], v[4:7]\n\t"
    "v_mfma_f32_16x16x32_bf16 v[4:7], a[120:123], a[72:75], v[4:7]\n\t"
    "v_mfma_f32_16x16x32_bf16 v[4:7], a[124:127], a[76:79], v[4:7]\n\t"
    "v_mfma_f32_16x16x32_bf16 v[8:11], a[96:99], a[80:83], v[44:47]\n\t"
    "ds_read_b128 a[96:99], v48 offset:57344\n\t"
    "v_mfma_f32_16x16x32_bf16 v[8:11], a[100:103], a[84:87], v[8:11]\n\t"
    "ds_read_b128 a[100:103], v49 offset:57344\n\t"
    "v_mfma_f32_16x16x32_bf16 v[8:11], a[104:107], a[88:91], v[8:11]\n\t"
    "ds_read_b128 a[104:107], v48 offset:58368\n\t"
    "v_mfma_f32_16x16x32_bf16 v[8:11], a[108:111], a[92:95], v[8:11]\n\t"
    "ds_read_b128 a[108:111], v49 offset:58368\n\t"
    "v_mfma_f32_16x16x32_bf16 v[12:15], a[112:115], a[80:83], v[44:47]\n\t"
    "ds_read_b128 a[112:115], v48 offset:61440\n\t"
    "v_mfma_f32_16x16x32_bf16 v[12:15], a[116:119], a[84:87], v[12:15]\n\t"
    "ds_read_b128 a[116:119], v49 offset:61440\n\t"
    "v_mfma_f32_16x16x32_bf16 v[12:15], a[120:123], a[88:91], v[12:15]\n\t"
    "ds_read_b128 a[120:123], v48 offset:62464\n\t"
    "v_mfma_f32_16x16x32_bf16 v[12:15], a[124:127], a[92:95], v[12:15]\n\t"
    "ds_read_b128 a[124:127], v49 offset:62464\n\t"
    "v_mfma_f32_16x16x32_bf16 v[128:131], a[128:131], v[16:19], v[128:131]\n\t"
    "v_exp_f32 v0, v0\n\t"
    "v_mfma_f32_16x16x32_bf16 v[132:135], a[132:135], v[16:19], v[132:135]\n\t"
    "v_exp_f32 v1, v1\n\t"
    "v_mfma_f32_16x16x32_bf16 v[136:139], a[136:139], v[16:19], v[136:139]\n\t"
    "v_exp_f32 v2, v2\n\t"
    "v_mfma_f32_16x16x32_bf16 v[140:143], a[140:143], v[16:19], v[140:143]\n\t"
    "v_exp_f32 v3, v3\n\t"
    "v_mfma_f32_16x16x32_bf16 v[144:147], a[144:147], v[16:19], v[144:147]\n\t"
    "v_exp_f32 v4, v4\n\t"
    "v_mfma_f32_16x16x32_bf16 v[148:151], a[148:151], v[16:19], v[148:151]\n\t"
    "v_exp_f32 v5, v5\n\t"
    "v_mfma_f32_16x16x32_bf16 v[152:155], a[152:155], v[16:19], v[152:155]\n\t"
    "v_exp_f32 v6, v6\n\t"
    "v_mfma_f32_16x16x32_bf16 v[156:159], a[156:159], v[16:19], v[156:159]\n\t"
    "v_exp_f32 v7, v7\n\t"
    "v_mfma_f32_16x16x32_bf16 a[168:171], a[184:187], v[16:19], a[168:171]\n\t"
    "v_cvt_pk_bf16_f32 v16, v0, v1\n\t"
    "v_cvt_pk_bf16_f32 v17, v2, v3\n\t"
    "v_mfma_f32_16x16x32_bf16 v[160:163], a[128:131], v[20:23], v[160:163]\n\t"
    "v_cvt_pk_bf16_f32 v18, v4, v5\n\t"
    "v_cvt_pk_bf16_f32 v19, v6, v7\n\t"
    "v_mfma_f32_16x16x32_bf16 v[164:167], a[132:135], v[20:23], v[164:167]\n\t"
    "v_exp_f32 v8, v8\n\t"
    "v_mfma_f32_16x16x32_bf16 v[168:171], a[136:139], v[20:23], v[168:171]\n\t"
    "v_exp_f32 v9, v9\n\t"
    "v_mfma_f32_16x16x32_bf16 v[172:175], a[140:143], v[20:23], v[172:175]\n\t"
    "v_exp_f32 v10, v10\n\t"
    "v_mfma_f32_16x16x32_bf16 v[176:179], a[144:147], v[20:23], v[176:179]\n\t"
    "v_exp_f32 v11, v11\n\t"
    "v_mfma_f32_16x16x32_bf16 v[180:183], a[148:151], v[20:23], v[180:183]\n\t"
    "v_exp_f32 v12, v12\n\t"
    "v_mfma_f32_16x16x32_bf16 v[184:187], a[152:155], v[20:23], v[184:187]\n\t"
    "v_exp_f32 v13, v13\n\t"
    "v_mfma_f32_16x16x32_bf16 v[188:191], a[156:159], v[20:23], v[188:191]\n\t"
    "v_exp_f32 v14, v14\n\t"
    "v_mfma_f32_16x16x32_bf16 a[172:175], a[184:187], v[20:23], a[172:175]\n\t"
    "v_cvt_pk_bf16_f32 v20, v8, v9\n\t"
    "v_cvt_pk_bf16_f32 v21, v10, v11\n\t"
    "s_waitcnt lgkmcnt(0)\n\t"
    "v_mfma_f32_16x16x32_bf16 v[0:3], a[96:99], a[0:3], v[24:27]\n\t"
    "v_exp_f32 v15, v15\n\t"
    "v_mfma_f32_16x16x32_bf16 v[0:3], a[100:103], a[4:7], v[0:3]\n\t"
    "v_cvt_pk_bf16_f32 v22, v12, v13\n\t"
    "v_cvt_pk_bf16_f32 v23, v14, v15\n\t"
    "v_mfma_f32_16x16x32_bf16 v[0:3], a[104:107], a[8:11], v[0:3]\n\t"
    "v_mfma_f32_16x16x32_bf16 v[0:3], a[108:111], a[12:15], v[0:3]\n\t"
    "v_mfma_f32_16x16x32_bf16 v[4:7], a[112:115], a[0:3], v[24:27]\n\t"
    "v_mfma_f32_16x16x32_bf16 v[4:7], a[116:119], a[4:7], v[4:7]\n\t"
    "v_mfma_f32_16x16x32_bf16 v[4:7], a[120:123], a[8:11], v[4:7]\n\t"
    "v_mfma_f32_16x16x32_bf16 v[4:7], a[124:127], a[12:15], v[4:7]\n\t"
    "v_mfma_f32_16x16x32_bf16 v[8:11], a[96:99], a[16:19], v[28:31]\n\t"
    "v_mfma_f32_16x16x32_bf16 v[8:11], a[100:103], a[20:23], v[8:11]\n\t"
    "v_mfma_f32_16x16x32_bf16 v[8:11], a[104:107], a[24:27], v[8:11]\n\t"
    "v_mfma_f32_16x16x32_bf16 v[8:11], a[108:111], a[28:31], v[8:11]\n\t"
    "v_exp_f32 v0, v0\n\t"
    "v_mfma_f32_16x16x32_bf16 v[12:15], a[112:115], a[16:19], v[28:31]\n\t"
    "v_exp_f32 v1, v1\n\t"
    "v_mfma_f32_16x16x32_bf16 v[12:15], a[116:119], a[20:23], v[12:15]\n\t"
    "v_exp_f32 v2, v2\n\t"
    "v_mfma_f32_16x16x32_bf16 v[12:15], a[120:123], a[24:27], v[12:15]\n\t"
    "v_exp_f32 v3, v3\n\t"
    "v_mfma_f32_16x16x32_bf16 v[12:15], a[124:127], a[28:31], v[12:15]\n\t"
    "s_waitcnt vmcnt(8)\n\t"
    "s_barrier\n\t"
    "v_mfma_f32_16x16x32_bf16 v[192:195], a[128:131], v[16:19], v[192:195]\n\t"
    "ds_read_b128 a[96:99], v48 offset:0\n\t"
    "ds_read_b128 a[100:103], v49 offset:0\n\t"
    "v_mfma_f32_16x16x32_bf16 v[196:199], a[132:135], v[16:19], v[196:199]\n\t"
    "ds_read_b128 a[104:107], v48 offset:1024\n\t"
    "ds_read_b128 a[108:111], v49 offset:1024\n\t"
    "v_mfma_f32_16x16x32_bf16 v[200:203], a[136:139], v[16:19], v[200:203]\n\t"
    "ds_read_b128 a[112:115], v48 offset:4096\n\t"
    "ds_read_b128 a[116:119], v49 offset:4096\n\t"
    "v_mfma_f32_16x16x32_bf16 v[204:207], a[140:143], v[16:19], v[204:207]\n\t"
    "ds_read_b128 a[120:123], v48 offset:5120\n\t"
    "ds_read_b128 a[124:127], v49 offset:5120\n\t"
    "v_mfma_f32_16x16x32_bf16 v[208:211], a[144:147], v[16:19], v[208:211]\n\t"
    "v_exp_f32 v4, v4\n\t"
    "v_mfma_f32_16x16x32_bf16 v[212:215], a[148:151], v[16:19], v[212:215]\n\t"
    "v_exp_f32 v5, v5\n\t"
    "v_mfma_f32_16x16x32_bf16 v[216:219], a[152:155], v[16:19], v[216:219]\n\t"
    "v_exp_f32 v6, v6\n\t"
    "v_mfma_f32_16x16x32_bf16 v[220:223], a[156:159], v[16:19], v[220:223]\n\t"
    "v_exp_f32 v7, v7\n\t"
    "v_mfma_f32_16x16x32_bf16 a[176:179], a[184:187], v[16:19], a[176:179]\n\t"
    "v_cvt_pk_bf16_f32 v16, v0, v1\n\t"
    "v_cvt_pk_bf16_f32 v17, v2, v3\n\t"
    "v_mfma_f32_16x16x32_bf16 v[224:227], a[128:131], v[20:23], v[224:227]\n\t"
    "v_cvt_pk_bf16_f32 v18, v4, v5\n\t"
    "v_cvt_pk_bf16_f32 v19, v6, v7\n\t"
    "v_mfma_f32_16x16x32_bf16 v[228:231], a[132:135], v[20:23], v[228:231]\n\t"
    "v_exp_f32 v8, v8\n\t"
    "v_mfma_f32_16x16x32_bf16 v[232:235], a[136:139], v[20:23], v[232:235]\n\t"
    "v_exp_f32 v9, v9\n\t"
    "v_mfma_f32_16x16x32_bf16 v[236:239], a[140:143], v[20:23], v[236:239]\n\t"
    "v_exp_f32 v10, v10\n\t"
    "v_mfma_f32_16x16x32_bf16 v[240:243], a[144:147], v[20:23], v[240:243]\n\t"
    "v_exp_f32 v11, v11\n\t"
    "v_mfma_f32_16x16x32_bf16 v[244:247], a[148:151], v[20:23], v[244:247]\n\t"
    "ds_read_b64_tr_b16 a[128:129], v50 offset:57344\n\t"
    "ds_read_b64_tr_b16 a[130:131], v50 offset:61440\n\t"
    "v_mfma_f32_16x16x32_bf16 v[248:251], a[152:155], v[20:23], v[248:251]\n\t"
    "ds_read_b64_tr_b16 a[132:133], v50 offset:57600\n\t"
    "ds_read_b64_tr_b16 a[134:135], v50 offset:61696\n\t"
    "v_mfma_f32_16x16x32_bf16 v[252:255], a[156:159], v[20:23], v[252:255]\n\t"
    "ds_read_b64_tr_b16 a[136:137], v50 offset:57856\n\t"
    "ds_read_b64_tr_b16 a[138:139], v50 offset:61952\n\t"
    "v_mfma_f32_16x16x32_bf16 a[180:183], a[184:187], v[20:23], a[180:183]\n\t"
    "ds_read_b64_tr_b16 a[140:141], v50 offset:58112\n\t"
    "v_cvt_pk_bf16_f32 v20, v8, v9\n\t"
    "s_waitcnt lgkmcnt(7)\n\t"
    "v_mfma_f32_16x16x32_bf16 v[0:3], a[96:99], a[0:3], v[24:27]\n\t"
    "ds_read_b64_tr_b16 a[142:143], v50 offset:62208\n\t"
    "v_cvt_pk_bf16_f32 v21, v10, v11\n\t"
    "v_mfma_f32_16x16x32_bf16 v[0:3], a[100:103], a[4:7], v[0:3]\n\t"
    "v_exp_f32 v12, v12\n\t"
    "v_mfma_f32_16x16x32_bf16 v[0:3], a[104:107], a[8:11], v[0:3]\n\t"
    "v_exp_f32 v13, v13\n\t"
    "v_mfma_f32_16x16x32_bf16 v[0:3], a[108:111], a[12:15], v[0:3]\n\t"
    "v_exp_f32 v14, v14\n\t"
    "v_mfma_f32_16x16x32_bf16 v[4:7], a[112:115], a[0:3], v[24:27]\n\t"
    "v_exp_f32 v15, v15\n\t"
    "v_mfma_f32_16x16x32_bf16 v[4:7], a[116:119], a[4:7], v[4:7]\n\t"
    "ds_read_b64_tr_b16 a[144:145], v50 offset:58368\n\t"
    "v_cvt_pk_bf16_f32 v22, v12, v13\n\t"
    "v_mfma_f32_16x16x32_bf16 v[4:7], a[120:123], a[8:11], v[4:7]\n\t"
    "ds_read_b64_tr_b16 a[146:147], v50 offset:62464\n\t"
    "v_cvt_pk_bf16_f32 v23, v14, v15\n\t"
    "v_mfma_f32_16x16x32_bf16 v[4:7], a[124:127], a[12:15], v[4:7]\n\t"
    "ds_read_b64_tr_b16 a[148:149], v50 offset:58624\n\t"
    "ds_read_b64_tr_b16 a[150:151], v50 offset:62720\n\t"
    "v_mfma_f32_16x16x32_bf16 v[8:11], a[96:99], a[16:19], v[28:31]\n\t"
    "ds_read_b64_tr_b16 a[152:153], v50 offset:58880\n\t"
    "ds_read_b64_tr_b16 a[154:155], v50 offset:62976\n\t"
    "v_mfma_f32_16x16x32_bf16 v[8:11], a[100:103], a[20:23], v[8:11]\n\t"
    "ds_read_b64_tr_b16 a[156:157], v50 offset:59136\n\t"
    "ds_read_b64_tr_b16 a[158:159], v50 offset:63232\n\t"
    "v_mfma_f32_16x16x32_bf16 v[8:11], a[104:107], a[24:27], v[8:11]\n\t"
    "ds_read_b128 a[96:99], v48 offset:8192\n\t"
    "ds_read_b128 a[100:103], v49 offset:8192\n\t"
    "v_mfma_f32_16x16x32_bf16 v[8:11], a[108:111], a[28:31], v[8:11]\n\t"
    "ds_read_b128 a[104:107], v48 offset:9216\n\t"
    "ds_read_b128 a[108:111], v49 offset:9216\n\t"
    "v_mfma_f32_16x16x32_bf16 v[12:15], a[112:115], a[16:19], v[28:31]\n\t"
    "ds_read_b128 a[112:115], v48 offset:12288\n\t"
    "v_mfma_f32_16x16x32_bf16 v[12:15], a[116:119], a[20:23], v[12:15]\n\t"
    "ds_read_b128 a[116:119], v49 offset:12288\n\t"
    "v_mfma_f32_16x16x32_bf16 v[12:15], a[120:123], a[24:27], v[12:15]\n\t"
    "ds_read_b128 a[120:123], v48 offset:13312\n\t"
    "v_mfma_f32_16x16x32_bf16 v[12:15], a[124:127], a[28:31], v[12:15]\n\t"
    "ds_read_b128 a[124:127], v49 offset:13312\n\t"
    "s_waitcnt lgkmcnt(13)\n\t"
    "v_mfma_f32_16x16x32_bf16 v[64:67], a[128:131], v[16:19], v[64:67]\n\t"
    "v_exp_f32 v0, v0\n\t"
    "v_mfma_f32_16x16x32_bf16 v[68:71], a[132:135], v[16:19], v[68:71]\n\t"
    "v_exp_f32 v1, v1\n\t"
    "v_mfma_f32_16x16x32_bf16 v[72:75], a[136:139], v[16:19], v[72:75]\n\t"
    "v_exp_f32 v2, v2\n\t"
    "v_mfma_f32_16x16x32_bf16 v[76:79], a[140:143], v[16:19], v[76:79]\n\t"
    "v_exp_f32 v3, v3\n\t"
    "v_mfma_f32_16x16x32_bf16 v[80:83], a[144:147], v[16:19], v[80:83]\n\t"
    "v_exp_f32 v4, v4\n\t"
    "s_waitcnt lgkmcnt(3)\n\t"
    "v_mfma_f32_16x16x32_bf16 v[84:87], a[148:151], v[16:19], v[84:87]\n\t"
    "v_exp_f32 v5, v5\n\t"
    "v_mfma_f32_16x16x32_bf16 v[88:91], a[152:155], v[16:19], v[88:91]\n\t"
    "v_exp_f32 v6, v6\n\t"
    "v_mfma_f32_16x16x32_bf16 v[92:95], a[156:159], v[16:19], v[92:95]\n\t"
    "v_exp_f32 v7, v7\n\t"
    "v_mfma_f32_16x16x32_bf16 a[160:163], a[184:187], v[16:19], a[160:163]\n\t"
    "v_cvt_pk_bf16_f32 v16, v0, v1\n\t"
    "v_cvt_pk_bf16_f32 v17, v2, v3\n\t"
    "v_mfma_f32_16x16x32_bf16 v[96:99], a[128:131], v[20:23], v[96:99]\n\t"
    "v_cvt_pk_bf16_f32 v18, v4, v5\n\t"
    "v_cvt_pk_bf16_f32 v19, v6, v7\n\t"
    "v_mfma_f32_16x16x32_bf16 v[100:103], a[132:135], v[20:23], v[100:103]\n\t"
    "v_exp_f32 v8, v8\n\t"
    "v_mfma_f32_16x16x32_bf16 v[104:107], a[136:139], v[20:23], v[104:107]\n\t"
    "v_exp_f32 v9, v9\n\t"
    "v_mfma_f32_16x16x32_bf16 v[108:111], a[140:143], v[20:23], v[108:111]\n\t"
    "v_exp_f32 v10, v10\n\t"
    "v_mfma_f32_16x16x32_bf16 v[112:115], a[144:147], v[20:23], v[112:115]\n\t"
    "v_exp_f32 v11, v11\n\t"
    "v_mfma_f32_16x16x32_bf16 v[116:119], a[148:151], v[20:23], v[116:119]\n\t"
    "ds_read_b64_tr_b16 a[128:129], v50 offset:0\n\t"
    "ds_read_b64_tr_b16 a[130:131], v50 offset:4096\n\t"
    "v_mfma_f32_16x16x32_bf16 v[120:123], a[152:155], v[20:23], v[120:123]\n\t"
    "ds_read_b64_tr_b16 a[132:133], v50 offset:256\n\t"
    "ds_read_b64_tr_b16 a[134:135], v50 offset:4352\n\t"
    "v_mfma_f32_16x16x32_bf16 v[124:127], a[156:159], v[20:23], v[124:127]\n\t"
    "ds_read_b64_tr_b16 a[136:137], v50 offset:512\n\t"
    "ds_read_b64_tr_b16 a[138:139], v50 offset:4608\n\t"
    "v_mfma_f32_16x16x32_bf16 a[164:167], a[184:187], v[20:23], a[164:167]\n\t"
    "ds_read_b64_tr_b16 a[140:141], v50 offset:768\n\t"
    "v_cvt_pk_bf16_f32 v20, v8, v9\n\t"
    "v_mfma_f32_16x16x32_bf16 v[0:3], a[96:99], a[0:3], v[24:27]\n\t"
    "ds_read_b64_tr_b16 a[142:143], v50 offset:4864\n\t"
    "v_cvt_pk_bf16_f32 v21, v10, v11\n\t"
    "v_mfma_f32_16x16x32_bf16 v[0:3], a[100:103], a[4:7], v[0:3]\n\t"
    "v_exp_f32 v12, v12\n\t"
    "v_mfma_f32_16x16x32_bf16 v[0:3], a[104:107], a[8:11], v[0:3]\n\t"
    "v_exp_f32 v13, v13\n\t"
    "v_mfma_f32_16x16x32_bf16 v[0:3], a[108:111], a[12:15], v[0:3]\n\t"
    "v_exp_f32 v14, v14\n\t"
    "v_mfma_f32_16x16x32_bf16 v[4:7], a[112:115], a[0:3], v[24:27]\n\t"
    "v_exp_f32 v15, v15\n\t"
    "s_waitcnt lgkmcnt(6)\n\t"
    "v_mfma_f32_16x16x32_bf16 v[4:7], a[116:119], a[4:7], v[4:7]\n\t"
    "ds_read_b64_tr_b16 a[144:145], v50 offset:1024\n\t"
    "v_cvt_pk_bf16_f32 v22, v12, v13\n\t"
    "v_mfma_f32_16x16x32_bf16 v[4:7], a[120:123], a[8:11], v[4:7]\n\t"
    "ds_read_b64_tr_b16 a[146:147], v50 offset:5120\n\t"
    "v_cvt_pk_bf16_f32 v23, v14, v15\n\t"
    "v_mfma_f32_16x16x32_bf16 v[4:7], a[124:127], a[12:15], v[4:7]\n\t"
    "ds_read_b64_tr_b16 a[148:149], v50 offset:1280\n\t"
    "ds_read_b64_tr_b16 a[150:151], v50 offset:5376\n\t"
    "v_mfma_f32_16x16x32_bf16 v[8:11], a[96:99], a[16:19], v[28:31]\n\t"
    "ds_read_b64_tr_b16 a[152:153], v50 offset:1536\n\t"
    "ds_read_b64_tr_b16 a[154:155], v50 offset:5632\n\t"
    "v_mfma_f32_16x16x32_bf16 v[8:11], a[100:103], a[20:23], v[8:11]\n\t"
    "ds_read_b64_tr_b16 a[156:157], v50 offset:1792\n\t"
    "ds_read_b64_tr_b16 a[158:159], v50 offset:5888\n\t"
    "v_mfma_f32_16x16x32_bf16 v[8:11], a[104:107], a[24:27], v[8:11]\n\t"
    "v_exp_f32 v0, v0\n\t"
    "v_mfma_f32_16x16x32_bf16 v[8:11], a[108:111], a[28:31], v[8:11]\n\t"
    "v_exp_f32 v1, v1\n\t"
    "v_mfma_f32_16x16x32_bf16 v[12:15], a[112:115], a[16:19], v[28:31]\n\t"
    "v_exp_f32 v2, v2\n\t"
    "v_mfma_f32_16x16x32_bf16 v[12:15], a[116:119], a[20:23], v[12:15]\n\t"
    "v_exp_f32 v3, v3\n\t"
    "v_mfma_f32_16x16x32_bf16 v[12:15], a[120:123], a[24:27], v[12:15]\n\t"
    "v_exp_f32 v4, v4\n\t"
    "v_mfma_f32_16x16x32_bf16 v[12:15], a[124:127], a[28:31], v[12:15]\n\t"
    "s_waitcnt vmcnt(0)\n\t"
    "s_barrier\n\t"
    "v_mfma_f32_16x16x32_bf16 v[64:67], a[128:131], v[16:19], v[64:67]\n\t"
    "ds_read_b128 a[96:99], v48 offset:16384\n\t"
    "ds_read_b128 a[100:103], v49 offset:16384\n\t"
    "s_waitcnt lgkmcnt(4)\n\t"
    "v_mfma_f32_16x16x32_bf16 v[68:71], a[132:135], v[16:19], v[68:71]\n\t"
    "ds_read_b128 a[104:107], v48 offset:17408\n\t"
    "ds_read_b128 a[108:111], v49 offset:17408\n\t"
    "v_mfma_f32_16x16x32_bf16 v[72:75], a[136:139], v[16:19], v[72:75]\n\t"
    "ds_read_b128 a[112:115], v48 offset:20480\n\t"
    "ds_read_b128 a[116:119], v49 offset:20480\n\t"
    "v_mfma_f32_16x16x32_bf16 v[76:79], a[140:143], v[16:19], v[76:79]\n\t"
    "ds_read_b128 a[120:123], v48 offset:21504\n\t"
    "ds_read_b128 a[124:127], v49 offset:21504\n\t"
    "v_mfma_f32_16x16x32_bf16 v[80:83], a[144:147], v[16:19], v[80:83]\n\t"
    "v_exp_f32 v5, v5\n\t"
    "v_mfma_f32_16x16x32_bf16 v[84:87], a[148:151], v[16:19], v[84:87]\n\t"
    "v_exp_f32 v6, v6\n\t"
    "v_mfma_f32_16x16x32_bf16 v[88:91], a[152:155], v[16:19], v[88:91]\n\t"
    "v_exp_f32 v7, v7\n\t"
    "s_waitcnt lgkmcnt(8)\n\t"
    "v_mfma_f32_16x16x32_bf16 v[92:95], a[156:159], v[16:19], v[92:95]\n\t"
    "v_exp_f32 v8, v8\n\t"
    "v_mfma_f32_16x16x32_bf16 a[160:163], a[184:187], v[16:19], a[160:163]\n\t"
    "v_cvt_pk_bf16_f32 v16, v0, v1\n\t"
    "v_cvt_pk_bf16_f32 v17, v2, v3\n\t"
    "v_mfma_f32_16x16x32_bf16 v[96:99], a[128:131], v[20:23], v[96:99]\n\t"
    "ds_read_b64_tr_b16 a[128:129], v50 offset:8192\n\t"
    "v_cvt_pk_bf16_f32 v18, v4, v5\n\t"
    "v_mfma_f32_16x16x32_bf16 v[100:103], a[132:135], v[20:23], v[100:103]\n\t"
    "ds_read_b64_tr_b16 a[130:131], v50 offset:12288\n\t"
    "v_cvt_pk_bf16_f32 v19, v6, v7\n\t"
    "v_mfma_f32_16x16x32_bf16 v[104:107], a[136:139], v[20:23], v[104:107]\n\t"
    "v_exp_f32 v9, v9\n\t"
    "v_mfma_f32_16x16x32_bf16 v[108:111], a[140:143], v[20:23], v[108:111]\n\t"
    "v_exp_f32 v10, v10\n\t"
    "v_mfma_f32_16x16x32_bf16 v[112:115], a[144:147], v[20:23], v[112:115]\n\t"
    "v_exp_f32 v11, v11\n\t"
    "v_mfma_f32_16x16x32_bf16 v[116:119], a[148:151], v[20:23], v[116:119]\n\t"
    "ds_read_b64_tr_b16 a[132:133], v50 offset:8448\n\t"
    "ds_read_b64_tr_b16 a[134:135], v50 offset:12544\n\t"
    "v_mfma_f32_16x16x32_bf16 v[120:123], a[152:155], v[20:23], v[120:123]\n\t"
    "ds_read_b64_tr_b16 a[136:137], v50 offset:8704\n\t"
    "ds_read_b64_tr_b16 a[138:139], v50 offset:12800\n\t"
    "v_mfma_f32_16x16x32_bf16 v[124:127], a[156:159], v[20:23], v[124:127]\n\t"
    "ds_read_b64_tr_b16 a[140:141], v50 offset:8960\n\t"
    "ds_read_b64_tr_b16 a[142:143], v50 offset:13056\n\t"
    "v_mfma_f32_16x16x32_bf16 a[164:167], a[184:187], v[20:23], a[164:167]\n\t"
    "v_cvt_pk_bf16_f32 v20, v8, v9\n\t"
    "v_cvt_pk_bf16_f32 v21, v10, v11\n\t"
    "s_waitcnt lgkmcnt(7)\n\t"
    "v_mfma_f32_16x16x32_bf16 v[0:3], a[96:99], a[0:3], v[24:27]\n\t"
    "v_exp_f32 v12, v12\n\t"
    "v_mfma_f32_16x16x32_bf16 v[0:3], a[100:103], a[4:7], v[0:3]\n\t"
    "v_exp_f32 v13, v13\n\t"
    "v_mfma_f32_16x16x32_bf16 v[0:3], a[104:107], a[8:11], v[0:3]\n\t"
    "v_exp_f32 v14, v14\n\t"
    "v_mfma_f32_16x16x32_bf16 v[0:3], a[108:111], a[12:15], v[0:3]\n\t"
    "v_exp_f32 v15, v15\n\t"
    "v_mfma_f32_16x16x32_bf16 v[4:7], a[112:115], a[0:3], v[24:27]\n\t"
    "ds_read_b64_tr_b16 a[144:145], v50 offset:9216\n\t"
    "v_cvt_pk_bf16_f32 v22, v12, v13\n\t"
    "v_mfma_f32_16x16x32_bf16 v[4:7], a[116:119], a[4:7], v[4:7]\n\t"
    "ds_read_b64_tr_b16 a[146:147], v50 offset:13312\n\t"
    "v_cvt_pk_bf16_f32 v23, v14, v15\n\t"
    "v_mfma_f32_16x16x32_bf16 v[4:7], a[120:123], a[8:11], v[4:7]\n\t"
    "ds_read_b64_tr_b16 a[148:149], v50 offset:9472\n\t"
    "ds_read_b64_tr_b16 a[150:151], v50 offset:13568\n\t"
    "v_mfma_f32_16x16x32_bf16 v[4:7], a[124:127], a[12:15], v[4:7]\n\t"
    "ds_read_b64_tr_b16 a[152:153], v50 offset:9728\n\t"
    "ds_read_b64_tr_b16 a[154:155], v50 offset:13824\n\t"
    "v_mfma_f32_16x16x32_bf16 v[8:11], a[96:99], a[16:19], v[28:31]\n\t"
    "ds_read_b64_tr_b16 a[156:157], v50 offset:9984\n\t"
    "ds_read_b64_tr_b16 a[158:159], v50 offset:14080\n\t"
    "v_mfma_f32_16x16x32_bf16 v[8:11], a[100:103], a[20:23], v[8:11]\n\t"
    "ds_read_b128 a[96:99], v48 offset:24576\n\t"
    "ds_read_b128 a[100:103], v49 offset:24576\n\t"
    "v_mfma_f32_16x16x32_bf16 v[8:11], a[104:107], a[24:27], v[8:11]\n\t"
    "ds_read_b128 a[104:107], v48 offset:25600\n\t"
    "v_mfma_f32_16x16x32_bf16 v[8:11], a[108:111], a[28:31], v[8:11]\n\t"
    "ds_read_b128 a[108:111], v49 offset:25600\n\t"
    "v_mfma_f32_16x16x32_bf16 v[12:15], a[112:115], a[16:19], v[28:31]\n\t"
    "ds_read_b128 a[112:115], v48 offset:28672\n\t"
    "v_mfma_f32_16x16x32_bf16 v[12:15], a[116:119], a[20:23], v[12:15]\n\t"
    "ds_read_b128 a[116:119], v49 offset:28672\n\t"
    "v_mfma_f32_16x16x32_bf16 v[12:15], a[120:123], a[24:27], v[12:15]\n\t"
    "ds_read_b128 a[120:123], v48 offset:29696\n\t"
    "v_mfma_f32_16x16x32_bf16 v[12:15], a[124:127], a[28:31], v[12:15]\n\t"
    "ds_read_b128 a[124:127], v49 offset:29696\n\t"
    "s_waitcnt lgkmcnt(11)\n\t"
    "v_mfma_f32_16x16x32_bf16 v[64:67], a[128:131], v[16:19], v[64:67]\n\t"
    "v_exp_f32 v0, v0\n\t"
    "v_mfma_f32_16x16x32_bf16 v[68:71], a[132:135], v[16:19], v[68:71]\n\t"
    "v_exp_f32 v1, v1\n\t"
    "v_mfma_f32_16x16x32_bf16 v[72:75], a[136:139], v[16:19], v[72:75]\n\t"
    "v_exp_f32 v2, v2\n\t"
    "v_mfma_f32_16x16x32_bf16 v[76:79], a[140:143], v[16:19], v[76:79]\n\t"
    "v_exp_f32 v3, v3\n\t"
    "v_mfma_f32_16x16x32_bf16 v[80:83], a[144:147], v[16:19], v[80:83]\n\t"
    "v_exp_f32 v4, v4\n\t"
    "v_mfma_f32_16x16x32_bf16 v[84:87], a[148:151], v[16:19], v[84:87]\n\t"
    "v_exp_f32 v5, v5\n\t"
    "s_waitcnt lgkmcnt(2)\n\t"
    "v_mfma_f32_16x16x32_bf16 v[88:91], a[152:155], v[16:19], v[88:91]\n\t"
    "v_exp_f32 v6, v6\n\t"
    "v_mfma_f32_16x16x32_bf16 v[92:95], a[156:159], v[16:19], v[92:95]\n\t"
    "v_exp_f32 v7, v7\n\t"
    "v_mfma_f32_16x16x32_bf16 a[160:163], a[184:187], v[16:19], a[160:163]\n\t"
    "v_cvt_pk_bf16_f32 v16, v0, v1\n\t"
    "v_cvt_pk_bf16_f32 v17, v2, v3\n\t"
    "v_mfma_f32_16x16x32_bf16 v[96:99], a[128:131], v[20:23], v[96:99]\n\t"
    "v_cvt_pk_bf16_f32 v18, v4, v5\n\t"
    "v_cvt_pk_bf16_f32 v19, v6, v7\n\t"
    "v_mfma_f32_16x16x32_bf16 v[100:103], a[132:135], v[20:23], v[100:103]\n\t"
    "v_exp_f32 v8, v8\n\t"
    "v_mfma_f32_16x16x32_bf16 v[104:107], a[136:139], v[20:23], v[104:107]\n\t"
    "v_exp_f32 v9, v9\n\t"
    "v_mfma_f32_16x16x32_bf16 v[108:111], a[140:143], v[20:23], v[108:111]\n\t"
    "v_exp_f32 v10, v10\n\t"
    "v_mfma_f32_16x16x32_bf16 v[112:115], a[144:147], v[20:23], v[112:115]\n\t"
    "v_exp_f32 v11, v11\n\t"
    "v_mfma_f32_16x16x32_bf16 v[116:119], a[148:151], v[20:23], v[116:119]\n\t"
    "ds_read_b64_tr_b16 a[128:129], v50 offset:16384\n\t"
    "ds_read_b64_tr_b16 a[130:131], v50 offset:20480\n\t"
    "v_mfma_f32_16x16x32_bf16 v[120:123], a[152:155], v[20:23], v[120:123]\n\t"
    "ds_read_b64_tr_b16 a[132:133], v50 offset:16640\n\t"
    "ds_read_b64_tr_b16 a[134:135], v50 offset:20736\n\t"
    "v_mfma_f32_16x16x32_bf16 v[124:127], a[156:159], v[20:23], v[124:127]\n\t"
    "ds_read_b64_tr_b16 a[136:137], v50 offset:16896\n\t"
    "ds_read_b64_tr_b16 a[138:139], v50 offset:20992\n\t"
    "v_mfma_f32_16x16x32_bf16 a[164:167], a[184:187], v[20:23], a[164:167]\n\t"
    "ds_read_b64_tr_b16 a[140:141], v50 offset:17152\n\t"
    "v_cvt_pk_bf16_f32 v20, v8, v9\n\t"
    "v_mfma_f32_16x16x32_bf16 v[0:3], a[96:99], a[0:3], v[24:27]\n\t"
    "ds_read_b64_tr_b16 a[142:143], v50 offset:21248\n\t"
    "v_cvt_pk_bf16_f32 v21, v10, v11\n\t"
    "v_mfma_f32_16x16x32_bf16 v[0:3], a[100:103], a[4:7], v[0:3]\n\t"
    "v_exp_f32 v12, v12\n\t"
    "v_mfma_f32_16x16x32_bf16 v[0:3], a[104:107], a[8:11], v[0:3]\n\t"
    "v_exp_f32 v13, v13\n\t"
    "v_mfma_f32_16x16x32_bf16 v[0:3], a[108:111], a[12:15], v[0:3]\n\t"
    "v_exp_f32 v14, v14\n\t"
    "v_mfma_f32_16x16x32_bf16 v[4:7], a[112:115], a[0:3], v[24:27]\n\t"
    "v_exp_f32 v15, v15\n\t"
    "v_mfma_f32_16x16x32_bf16 v[4:7], a[116:119], a[4:7], v[4:7]\n\t"
    "ds_read_b64_tr_b16 a[144:145], v50 offset:17408\n\t"
    "v_cvt_pk_bf16_f32 v22, v12, v13\n\t"
    "s_waitcnt lgkmcnt(5)\n\t"
    "v_mfma_f32_16x16x32_bf16 v[4:7], a[120:123], a[8:11], v[4:7]\n\t"
    "ds_read_b64_tr_b16 a[146:147], v50 offset:21504\n\t"
    "v_cvt_pk_bf16_f32 v23, v14, v15\n\t"
    "v_mfma_f32_16x16x32_bf16 v[4:7], a[124:127], a[12:15], v[4:7]\n\t"
    "ds_read_b64_tr_b16 a[148:149], v50 offset:17664\n\t"
    "ds_read_b64_tr_b16 a[150:151], v50 offset:21760\n\t"
    "v_mfma_f32_16x16x32_bf16 v[8:11], a[96:99], a[16:19], v[28:31]\n\t"
    "ds_read_b64_tr_b16 a[152:153], v50 offset:17920\n\t"
    "ds_read_b64_tr_b16 a[154:155], v50 offset:22016\n\t"
    "v_mfma_f32_16x16x32_bf16 v[8:11], a[100:103], a[20:23], v[8:11]\n\t"
    "ds_read_b64_tr_b16 a[156:157], v50 offset:18176\n\t"
    "ds_read_b64_tr_b16 a[158:159], v50 offset:22272\n\t"
    "v_mfma_f32_16x16x32_bf16 v[8:11], a[104:107], a[24:27], v[8:11]\n\t"
    "ds_read_b128 a[96:99], v48 offset:57344\n\t"
    "ds_read_b128 a[100:103], v49 offset:57344\n\t"
    "v_mfma_f32_16x16x32_bf16 v[8:11], a[108:111], a[28:31], v[8:11]\n\t"
    "ds_read_b128 a[104:107], v48 offset:58368\n\t"
    "ds_read_b128 a[108:111], v49 offset:58368\n\t"
    "v_mfma_f32_16x16x32_bf16 v[12:15], a[112:115], a[16:19], v[28:31]\n\t"
    "ds_read_b128 a[112:115], v48 offset:61440\n\t"
    "v_mfma_f32_16x16x32_bf16 v[12:15], a[116:119], a[20:23], v[12:15]\n\t"
    "ds_read_b128 a[116:119], v49 offset:61440\n\t"
    "v_mfma_f32_16x16x32_bf16 v[12:15], a[120:123], a[24:27], v[12:15]\n\t"
    "ds_read_b128 a[120:123], v48 offset:62464\n\t"
    "v_mfma_f32_16x16x32_bf16 v[12:15], a[124:127], a[28:31], v[12:15]\n\t"
    "ds_read_b128 a[124:127], v49 offset:62464\n\t"
    "v_mfma_f32_16x16x32_bf16 v[64:67], a[128:131], v[16:19], v[64:67]\n\t"
    "v_exp_f32 v0, v0\n\t"
    "v_mfma_f32_16x16x32_bf16 v[68:71], a[132:135], v[16:19], v[68:71]\n\t"
    "v_exp_f32 v1, v1\n\t"
    "s_waitcnt lgkmcnt(8)\n\t"
    "v_mfma_f32_16x16x32_bf16 v[72:75], a[136:139], v[16:19], v[72:75]\n\t"
    "v_exp_f32 v2, v2\n\t"
    "v_mfma_f32_16x16x32_bf16 v[76:79], a[140:143], v[16:19], v[76:79]\n\t"
    "v_exp_f32 v3, v3\n\t"
    "v_mfma_f32_16x16x32_bf16 v[80:83], a[144:147], v[16:19], v[80:83]\n\t"
    "v_exp_f32 v4, v4\n\t"
    "v_mfma_f32_16x16x32_bf16 v[84:87], a[148:151], v[16:19], v[84:87]\n\t"
    "v_exp_f32 v5, v5\n\t"
    "v_mfma_f32_16x16x32_bf16 v[88:91], a[152:155], v[16:19], v[88:91]\n\t"
    "v_exp_f32 v6, v6\n\t"
    "v_mfma_f32_16x16x32_bf16 v[92:95], a[156:159], v[16:19], v[92:95]\n\t"
    "v_exp_f32 v7, v7\n\t"
    "v_mfma_f32_16x16x32_bf16 a[160:163], a[184:187], v[16:19], a[160:163]\n\t"
    "v_cvt_pk_bf16_f32 v16, v0, v1\n\t"
    "v_cvt_pk_bf16_f32 v17, v2, v3\n\t"
    "v_mfma_f32_16x16x32_bf16 v[96:99], a[128:131], v[20:23], v[96:99]\n\t"
    "v_cvt_pk_bf16_f32 v18, v4, v5\n\t"
    "v_cvt_pk_bf16_f32 v19, v6, v7\n\t"
    "v_mfma_f32_16x16x32_bf16 v[100:103], a[132:135], v[20:23], v[100:103]\n\t"
    "v_exp_f32 v8, v8\n\t"
    "v_mfma_f32_16x16x32_bf16 v[104:107], a[136:139], v[20:23], v[104:107]\n\t"
    "v_exp_f32 v9, v9\n\t"
    "v_mfma_f32_16x16x32_bf16 v[108:111], a[140:143], v[20:23], v[108:111]\n\t"
    "v_exp_f32 v10, v10\n\t"
    "v_mfma_f32_16x16x32_bf16 v[112:115], a[144:147], v[20:23], v[112:115]\n\t"
    "v_exp_f32 v11, v11\n\t"
    "v_mfma_f32_16x16x32_bf16 v[116:119], a[148:151], v[20:23], v[116:119]\n\t"
    "ds_read_b64_tr_b16 a[128:129], v50 offset:24576\n\t"
    "ds_read_b64_tr_b16 a[130:131], v50 offset:28672\n\t"
    "v_mfma_f32_16x16x32_bf16 v[120:123], a[152:155], v[20:23], v[120:123]\n\t"
    "ds_read_b64_tr_b16 a[132:133], v50 offset:24832\n\t"
    "ds_read_b64_tr_b16 a[134:135], v50 offset:28928\n\t"
    "v_mfma_f32_16x16x32_bf16 v[124:127], a[156:159], v[20:23], v[124:127]\n\t"
    "ds_read_b64_tr_b16 a[136:137], v50 offset:25088\n\t"
    "ds_read_b64_tr_b16 a[138:139], v50 offset:29184\n\t"
    "v_mfma_f32_16x16x32_bf16 a[164:167], a[184:187], v[20:23], a[164:167]\n\t"
    "ds_read_b64_tr_b16 a[140:141], v50 offset:25344\n\t"
    "v_cvt_pk_bf16_f32 v20, v8, v9\n\t"
    "s_waitcnt lgkmcnt(7)\n\t"
    "v_mfma_f32_16x16x32_bf16 v[0:3], a[96:99], a[32:35], v[32:35]\n\t"
    "ds_read_b64_tr_b16 a[142:143], v50 offset:29440\n\t"
    "v_cvt_pk_bf16_f32 v21, v10, v11\n\t"
    "v_mfma_f32_16x16x32_bf16 v[0:3], a[100:103], a[36:39], v[0:3]\n\t"
    "v_exp_f32 v12, v12\n\t"
    "v_mfma_f32_16x16x32_bf16 v[0:3], a[104:107], a[40:43], v[0:3]\n\t"
    "v_exp_f32 v13, v13\n\t"
    "v_mfma_f32_16x16x32_bf16 v[0:3], a[108:111], a[44:47], v[0:3]\n\t"
    "v_exp_f32 v14, v14\n\t"
    "v_mfma_f32_16x16x32_bf16 v[4:7], a[112:115], a[32:35], v[32:35]\n\t"
    "v_exp_f32 v15, v15\n\t"
    "v_mfma_f32_16x16x32_bf16 v[4:7], a[116:119], a[36:39], v[4:7]\n\t"
    "ds_read_b64_tr_b16 a[144:145], v50 offset:25600\n\t"
    "v_cvt_pk_bf16_f32 v22, v12, v13\n\t"
    "v_mfma_f32_16x16x32_bf16 v[4:7], a[120:123], a[40:43], v[4:7]\n\t"
    "ds_read_b64_tr_b16 a[146:147], v50 offset:29696\n\t"
    "v_cvt_pk_bf16_f32 v23, v14, v15\n\t"
    "v_mfma_f32_16x16x32_bf16 v[4:7], a[124:127], a[44:47], v[4:7]\n\t"
    "ds_read_b64_tr_b16 a[148:149], v50 offset:25856\n\t"
    "ds_read_b64_tr_b16 a[150:151], v50 offset:29952\n\t"
    "v_mfma_f32_16x16x32_bf16 v[8:11], a[96:99], a[48:51], v[36:39]\n\t"
    "ds_read_b64_tr_b16 a[152:153], v50 offset:26112\n\t"
    "ds_read_b64_tr_b16 a[154:155], v50 offset:30208\n\t"
    "v_mfma_f32_16x16x32_bf16 v[8:11], a[100:103], a[52:55], v[8:11]\n\t"
    "ds_read_b64_tr_b16 a[156:157], v50 offset:26368\n\t"
    "ds_read_b64_tr_b16 a[158:159], v50 offset:30464\n\t"
    "v_mfma_f32_16x16x32_bf16 v[8:11], a[104:107], a[56:59], v[8:11]\n\t"
    "ds_read_b128 a[96:99], v48 offset:0\n\t"
    "ds_read_b128 a[100:103], v49 offset:0\n\t"
    "v_mfma_f32_16x16x32_bf16 v[8:11], a[108:111], a[60:63], v[8:11]\n\t"
    "ds_read_b128 a[104:107], v48 offset:1024\n\t"
    "ds_read_b128 a[108:111], v49 offset:1024\n\t"
    "v_mfma_f32_16x16x32_bf16 v[12:15], a[112:115], a[48:51], v[36:39]\n\t"
    "ds_read_b128 a[112:115], v48 offset:4096\n\t"
    "v_mfma_f32_16x16x32_bf16 v[12:15], a[116:119], a[52:55], v[12:15]\n\t"
    "ds_read_b128 a[116:119], v49 offset:4096\n\t"
    "v_mfma_f32_16x16x32_bf16 v[12:15], a[120:123], a[56:59], v[12:15]\n\t"
    "ds_read_b128 a[120:123], v48 offset:5120\n\t"
    "v_mfma_f32_16x16x32_bf16 v[12:15], a[124:127], a[60:63], v[12:15]\n\t"
    "ds_read_b128 a[124:127], v49 offset:5120\n\t"
    "s_waitcnt lgkmcnt(13)\n\t"
    "v_mfma_f32_16x16x32_bf16 v[64:67], a[128:131], v[16:19], v[64:67]\n\t"
    "v_exp_f32 v0, v0\n\t"
    "v_mfma_f32_16x16x32_bf16 v[68:71], a[132:135], v[16:19], v[68:71]\n\t"
    "v_exp_f32 v1, v1\n\t"
    "v_mfma_f32_16x16x32_bf16 v[72:75], a[136:139], v[16:19], v[72:75]\n\t"
    "v_exp_f32 v2, v2\n\t"
    "v_mfma_f32_16x16x32_bf16 v[76:79], a[140:143], v[16:19], v[76:79]\n\t"
    "v_exp_f32 v3, v3\n\t"
    "v_mfma_f32_16x16x32_bf16 v[80:83], a[144:147], v[16:19], v[80:83]\n\t"
    "v_exp_f32 v4, v4\n\t"
    "s_waitcnt lgkmcnt(3)\n\t"
    "v_mfma_f32_16x16x32_bf16 v[84:87], a[148:151], v[16:19], v[84:87]\n\t"
    "v_exp_f32 v5, v5\n\t"
    "v_mfma_f32_16x16x32_bf16 v[88:91], a[152:155], v[16:19], v[88:91]\n\t"
    "v_exp_f32 v6, v6\n\t"
    "v_mfma_f32_16x16x32_bf16 v[92:95], a[156:159], v[16:19], v[92:95]\n\t"
    "v_exp_f32 v7, v7\n\t"
    "v_mfma_f32_16x16x32_bf16 a[160:163], a[184:187], v[16:19], a[160:163]\n\t"
    "v_cvt_pk_bf16_f32 v16, v0, v1\n\t"
    "v_cvt_pk_bf16_f32 v17, v2, v3\n\t"
    "v_mfma_f32_16x16x32_bf16 v[96:99], a[128:131], v[20:23], v[96:99]\n\t"
    "v_cvt_pk_bf16_f32 v18, v4, v5\n\t"
    "v_cvt_pk_bf16_f32 v19, v6, v7\n\t"
    "v_mfma_f32_16x16x32_bf16 v[100:103], a[132:135], v[20:23], v[100:103]\n\t"
    "v_exp_f32 v8, v8\n\t"
    "v_mfma_f32_16x16x32_bf16 v[104:107], a[136:139], v[20:23], v[104:107]\n\t"
    "v_exp_f32 v9, v9\n\t"
    "v_mfma_f32_16x16x32_bf16 v[108:111], a[140:143], v[20:23], v[108:111]\n\t"
    "v_exp_f32 v10, v10\n\t"
    "v_mfma_f32_16x16x32_bf16 v[112:115], a[144:147], v[20:23], v[112:115]\n\t"
    "v_exp_f32 v11, v11\n\t"
    "v_mfma_f32_16x16x32_bf16 v[116:119], a[148:151], v[20:23], v[116:119]\n\t"
    "ds_read_b64_tr_b16 a[128:129], v50 offset:57344\n\t"
    "ds_read_b64_tr_b16 a[130:131], v50 offset:61440\n\t"
    "v_mfma_f32_16x16x32_bf16 v[120:123], a[152:155], v[20:23], v[120:123]\n\t"
    "ds_read_b64_tr_b16 a[132:133], v50 offset:57600\n\t"
    "ds_read_b64_tr_b16 a[134:135], v50 offset:61696\n\t"
    "v_mfma_f32_16x16x32_bf16 v[124:127], a[156:159], v[20:23], v[124:127]\n\t"
    "ds_read_b64_tr_b16 a[136:137], v50 offset:57856\n\t"
    "ds_read_b64_tr_b16 a[138:139], v50 offset:61952\n\t"
    "v_mfma_f32_16x16x32_bf16 a[164:167], a[184:187], v[20:23], a[164:167]\n\t"
    "ds_read_b64_tr_b16 a[140:141], v50 offset:58112\n\t"
    "v_cvt_pk_bf16_f32 v20, v8, v9\n\t"
    "v_mfma_f32_16x16x32_bf16 v[0:3], a[96:99], a[32:35], v[32:35]\n\t"
    "ds_read_b64_tr_b16 a[142:143], v50 offset:62208\n\t"
    "v_cvt_pk_bf16_f32 v21, v10, v11\n\t"
    "v_mfma_f32_16x16x32_bf16 v[0:3], a[100:103], a[36:39], v[0:3]\n\t"
    "v_exp_f32 v12, v12\n\t"
    "v_mfma_f32_16x16x32_bf16 v[0:3], a[104:107], a[40:43], v[0:3]\n\t"
    "v_exp_f32 v13, v13\n\t"
    "v_mfma_f32_16x16x32_bf16 v[0:3], a[108:111], a[44:47], v[0:3]\n\t"
    "v_exp_f32 v14, v14\n\t"
    "v_accvgpr_read_b32 v61, a160\n\t"
    "v_mfma_f32_16x16x32_bf16 v[4:7], a[112:115], a[32:35], v[32:35]\n\t"
    "v_exp_f32 v15, v15\n\t"
    "v_rcp_f32 v62, v61\n\t"
    "s_waitcnt lgkmcnt(5)\n\t"
    "v_mfma_f32_16x16x32_bf16 v[4:7], a[116:119], a[36:39], v[4:7]\n\t"
    "ds_read_b64_tr_b16 a[144:145], v50 offset:58368\n\t"
    "v_cvt_pk_bf16_f32 v22, v12, v13\n\t"
    "v_cmp_nle_f32_e64 s[60:61], v61, s72\n\t"
    "v_mfma_f32_16x16x32_bf16 v[4:7], a[120:123], a[40:43], v[4:7]\n\t"
    "ds_read_b64_tr_b16 a[146:147], v50 offset:62464\n\t"
    "v_cvt_pk_bf16_f32 v23, v14, v15\n\t"
    "v_mul_f32 v64, v64, v62\n\t"
    "v_mfma_f32_16x16x32_bf16 v[4:7], a[124:127], a[44:47], v[4:7]\n\t"
    "ds_read_b64_tr_b16 a[148:149], v50 offset:58624\n\t"
    "ds_read_b64_tr_b16 a[150:151], v50 offset:62720\n\t"
    "v_mul_f32 v65, v65, v62\n\t"
    "v_mfma_f32_16x16x32_bf16 v[8:11], a[96:99], a[48:51], v[36:39]\n\t"
    "ds_read_b64_tr_b16 a[152:153], v50 offset:58880\n\t"
    "ds_read_b64_tr_b16 a[154:155], v50 offset:62976\n\t"
    "v_mul_f32 v66, v66, v62\n\t"
    "v_mfma_f32_16x16x32_bf16 v[8:11], a[100:103], a[52:55], v[8:11]\n\t"
    "ds_read_b64_tr_b16 a[156:157], v50 offset:59136\n\t"
    "ds_read_b64_tr_b16 a[158:159], v50 offset:63232\n\t"
    "v_mul_f32 v67, v67, v62\n\t"
    "v_mfma_f32_16x16x32_bf16 v[8:11], a[104:107], a[56:59], v[8:11]\n\t"
    "ds_read_b128 a[96:99], v48 offset:8192\n\t"
    "ds_read_b128 a[100:103], v49 offset:8192\n\t"
    "v_cvt_pk_bf16_f32 v64, v64, v65\n\t"
    "v_cvt_pk_bf16_f32 v65, v66, v67\n\t"
    "v_mfma_f32_16x16x32_bf16 v[8:11], a[108:111], a[60:63], v[8:11]\n\t"
    "ds_read_b128 a[104:107], v48 offset:9216\n\t"
    "ds_read_b128 a[108:111], v49 offset:9216\n\t"
    "ds_write_b64 v55, v[64:65] offset:0\n\t"
    "v_mfma_f32_16x16x32_bf16 v[12:15], a[112:115], a[48:51], v[36:39]\n\t"
    "ds_read_b128 a[112:115], v48 offset:12288\n\t"
    "v_mul_f32 v68, v68, v62\n\t"
    "v_mfma_f32_16x16x32_bf16 v[12:15], a[116:119], a[52:55], v[12:15]\n\t"
    "ds_read_b128 a[116:119], v49 offset:12288\n\t"
    "v_mul_f32 v69, v69, v62\n\t"
    "v_mfma_f32_16x16x32_bf16 v[12:15], a[120:123], a[56:59], v[12:15]\n\t"
    "ds_read_b128 a[120:123], v48 offset:13312\n\t"
    "v_mul_f32 v70, v70, v62\n\t"
    "v_mfma_f32_16x16x32_bf16 v[12:15], a[124:127], a[60:63], v[12:15]\n\t"
    "ds_read_b128 a[124:127], v49 offset:13312\n\t"
    "v_mul_f32 v71, v71, v62\n\t"
    "v_mfma_f32_16x16x32_bf16 v[128:131], a[128:131], v[16:19], v[128:131]\n\t"
    "v_exp_f32 v0, v0\n\t"
    "v_cvt_pk_bf16_f32 v68, v68, v69\n\t"
    "s_waitcnt lgkmcnt(9)\n\t"
    "v_mfma_f32_16x16x32_bf16 v[132:135], a[132:135], v[16:19], v[132:135]\n\t"
    "v_exp_f32 v1, v1\n\t"
    "v_cvt_pk_bf16_f32 v69, v70, v71\n\t"
    "ds_write_b64 v56, v[68:69] offset:0\n\t"
    "v_mfma_f32_16x16x32_bf16 v[136:139], a[136:139], v[16:19], v[136:139]\n\t"
    "v_exp_f32 v2, v2\n\t"
    "v_mul_f32 v72, v72, v62\n\t"
    "v_mfma_f32_16x16x32_bf16 v[140:143], a[140:143], v[16:19], v[140:143]\n\t"
    "v_exp_f32 v3, v3\n\t"
    "v_mul_f32 v73, v73, v62\n\t"
    "v_mfma_f32_16x16x32_bf16 v[144:147], a[144:147], v[16:19], v[144:147]\n\t"
    "v_exp_f32 v4, v4\n\t"
    "v_mul_f32 v74, v74, v62\n\t"
    "v_mfma_f32_16x16x32_bf16 v[148:151], a[148:151], v[16:19], v[148:151]\n\t"
    "v_exp_f32 v5, v5\n\t"
    "v_mul_f32 v75, v75, v62\n\t"
    "v_mfma_f32_16x16x32_bf16 v[152:155], a[152:155], v[16:19], v[152:155]\n\t"
    "v_exp_f32 v6, v6\n\t"
    "v_cvt_pk_bf16_f32 v72, v72, v73\n\t"
    "v_mfma_f32_16x16x32_bf16 v[156:159], a[156:159], v[16:19], v[156:159]\n\t"
    "v_exp_f32 v7, v7\n\t"
    "v_cvt_pk_bf16_f32 v73, v74, v75\n\t"
    "v_mfma_f32_16x16x32_bf16 a[168:171], a[184:187], v[16:19], a[168:171]\n\t"
    "v_cvt_pk_bf16_f32 v16, v0, v1\n\t"
    "v_cvt_pk_bf16_f32 v17, v2, v3\n\t"
    "ds_write_b64 v57, v[72:73] offset:0\n\t"
    "v_mfma_f32_16x16x32_bf16 v[160:163], a[128:131], v[20:23], v[160:163]\n\t"
    "v_cvt_pk_bf16_f32 v18, v4, v5\n\t"
    "v_cvt_pk_bf16_f32 v19, v6, v7\n\t"
    "v_mul_f32 v76, v76, v62\n\t"
    "v_mul_f32 v77, v77, v62\n\t"
    "v_mfma_f32_16x16x32_bf16 v[164:167], a[132:135], v[20:23], v[164:167]\n\t"
    "v_exp_f32 v8, v8\n\t"
    "v_mul_f32 v78, v78, v62\n\t"
    "v_mfma_f32_16x16x32_bf16 v[168:171], a[136:139], v[20:23], v[168:171]\n\t"
    "v_exp_f32 v9, v9\n\t"
    "v_mul_f32 v79, v79, v62\n\t"
    "v_mfma_f32_16x16x32_bf16 v[172:175], a[140:143], v[20:23], v[172:175]\n\t"
    "v_exp_f32 v10, v10\n\t"
    "v_cvt_pk_bf16_f32 v76, v76, v77\n\t"
    "v_mfma_f32_16x16x32_bf16 v[176:179], a[144:147], v[20:23], v[176:179]\n\t"
    "v_exp_f32 v11, v11\n\t"
    "v_cvt_pk_bf16_f32 v77, v78, v79\n\t"
    "v_mfma_f32_16x16x32_bf16 v[180:183], a[148:151], v[20:23], v[180:183]\n\t"
    "ds_read_b64_tr_b16 a[128:129], v50 offset:0\n\t"
    "ds_read_b64_tr_b16 a[130:131], v50 offset:4096\n\t"
    "ds_write_b64 v58, v[76:77] offset:0\n\t"
    "v_mfma_f32_16x16x32_bf16 v[184:187], a[152:155], v[20:23], v[184:187]\n\t"
    "ds_read_b64_tr_b16 a[132:133], v50 offset:256\n\t"
    "ds_read_b64_tr_b16 a[134:135], v50 offset:4352\n\t"
    "v_mul_f32 v80, v80, v62\n\t"
    "v_mfma_f32_16x16x32_bf16 v[188:191], a[156:159], v[20:23], v[188:191]\n\t"
    "ds_read_b64_tr_b16 a[136:137], v50 offset:512\n\t"
    "ds_read_b64_tr_b16 a[138:139], v50 offset:4608\n\t"
    "v_mul_f32 v81, v81, v62\n\t"
    "v_mul_f32 v82, v82, v62\n\t"
    "v_mfma_f32_16x16x32_bf16 a[172:175], a[184:187], v[20:23], a[172:175]\n\t"
    "ds_read_b64_tr_b16 a[140:141], v50 offset:768\n\t"
    "v_cvt_pk_bf16_f32 v20, v8, v9\n\t"
    "v_mul_f32 v83, v83, v62\n\t"
    "s_waitcnt lgkmcnt(8)\n\t"
    "v_mfma_f32_16x16x32_bf16 v[0:3], a[96:99], a[32:35], v[32:35]\n\t"
    "ds_read_b64_tr_b16 a[142:143], v50 offset:4864\n\t"
    "v_cvt_pk_bf16_f32 v21, v10, v11\n\t"
    "v_cvt_pk_bf16_f32 v80, v80, v81\n\t"
    "v_mfma_f32_16x16x32_bf16 v[0:3], a[100:103], a[36:39], v[0:3]\n\t"
    "v_exp_f32 v12, v12\n\t"
    "v_cvt_pk_bf16_f32 v81, v82, v83\n\t"
    "v_mfma_f32_16x16x32_bf16 v[0:3], a[104:107], a[40:43], v[0:3]\n\t"
    "v_exp_f32 v13, v13\n\t"
    "ds_write_b64 v55, v[80:81] offset:128\n\t"
    "v_mfma_f32_16x16x32_bf16 v[0:3], a[108:111], a[44:47], v[0:3]\n\t"
    "v_exp_f32 v14, v14\n\t"
    "v_mul_f32 v84, v84, v62\n\t"
    "v_mfma_f32_16x16x32_bf16 v[4:7], a[112:115], a[32:35], v[32:35]\n\t"
    "v_exp_f32 v15, v15\n\t"
    "v_mul_f32 v85, v85, v62\n\t"
    "v_mfma_f32_16x16x32_bf16 v[4:7], a[116:119], a[36:39], v[4:7]\n\t"
    "ds_read_b64_tr_b16 a[144:145], v50 offset:1024\n\t"
    "v_cvt_pk_bf16_f32 v22, v12, v13\n\t"
    "v_mul_f32 v86, v86, v62\n\t"
    "v_mfma_f32_16x16x32_bf16 v[4:7], a[120:123], a[40:43], v[4:7]\n\t"
    "ds_read_b64_tr_b16 a[146:147], v50 offset:5120\n\t"
    "v_cvt_pk_bf16_f32 v23, v14, v15\n\t"
    "v_mul_f32 v87, v87, v62\n\t"
    "v_cvt_pk_bf16_f32 v84, v84, v85\n\t"
    "v_mfma_f32_16x16x32_bf16 v[4:7], a[124:127], a[44:47], v[4:7]\n\t"
    "ds_read_b64_tr_b16 a[148:149], v50 offset:1280\n\t"
    "ds_read_b64_tr_b16 a[150:151], v50 offset:5376\n\t"
    "v_cvt_pk_bf16_f32 v85, v86, v87\n\t"
    "v_mfma_f32_16x16x32_bf16 v[8:11], a[96:99], a[48:51], v[36:39]\n\t"
    "ds_read_b64_tr_b16 a[152:153], v50 offset:1536\n\t"
    "ds_read_b64_tr_b16 a[154:155], v50 offset:5632\n\t"
    "ds_write_b64 v56, v[84:85] offset:128\n\t"
    "v_mfma_f32_16x16x32_bf16 v[8:11], a[100:103], a[52:55], v[8:11]\n\t"
    "ds_read_b64_tr_b16 a[156:157], v50 offset:1792\n\t"
    "ds_read_b64_tr_b16 a[158:159], v50 offset:5888\n\t"
    "v_mul_f32 v88, v88, v62\n\t"
    "v_mfma_f32_16x16x32_bf16 v[8:11], a[104:107], a[56:59], v[8:11]\n\t"
    "ds_read_b128 a[96:99], v48 offset:16384\n\t"
    "ds_read_b128 a[100:103], v49 offset:16384\n\t"
    "v_mul_f32 v89, v89, v62\n\t"
    "v_mfma_f32_16x16x32_bf16 v[8:11], a[108:111], a[60:63], v[8:11]\n\t"
    "ds_read_b128 a[104:107], v48 offset:17408\n\t"
    "ds_read_b128 a[108:111], v49 offset:17408\n\t"
    "v_mul_f32 v90, v90, v62\n\t"
    "v_mfma_f32_16x16x32_bf16 v[12:15], a[112:115], a[48:51], v[36:39]\n\t"
    "ds_read_b128 a[112:115], v48 offset:20480\n\t"
    "v_mul_f32 v91, v91, v62\n\t"
    "v_mfma_f32_16x16x32_bf16 v[12:15], a[116:119], a[52:55], v[12:15]\n\t"
    "ds_read_b128 a[116:119], v49 offset:20480\n\t"
    "v_cvt_pk_bf16_f32 v88, v88, v89\n\t"
    "v_cvt_pk_bf16_f32 v89, v90, v91\n\t"
    "v_mfma_f32_16x16x32_bf16 v[12:15], a[120:123], a[56:59], v[12:15]\n\t"
    "ds_read_b128 a[120:123], v48 offset:21504\n\t"
    "ds_write_b64 v57, v[88:89] offset:128\n\t"
    "v_mfma_f32_16x16x32_bf16 v[12:15], a[124:127], a[60:63], v[12:15]\n\t"
    "ds_read_b128 a[124:127], v49 offset:21504\n\t"
    "v_mul_f32 v92, v92, v62\n\t"
    "s_waitcnt lgkmcnt(11)\n\t"
    "v_mfma_f32_16x16x32_bf16 v[128:131], a[128:131], v[16:19], v[128:131]\n\t"
    "v_exp_f32 v0, v0\n\t"
    "v_mul_f32 v93, v93, v62\n\t"
    "v_mfma_f32_16x16x32_bf16 v[132:135], a[132:135], v[16:19], v[132:135]\n\t"
    "v_exp_f32 v1, v1\n\t"
    "v_mul_f32 v94, v94, v62\n\t"
    "v_mfma_f32_16x16x32_bf16 v[136:139], a[136:139], v[16:19], v[136:139]\n\t"
    "v_exp_f32 v2, v2\n\t"
    "v_mul_f32 v95, v95, v62\n\t"
    "v_mfma_f32_16x16x32_bf16 v[140:143], a[140:143], v[16:19], v[140:143]\n\t"
    "v_exp_f32 v3, v3\n\t"
    "v_cvt_pk_bf16_f32 v92, v92, v93\n\t"
    "v_mfma_f32_16x16x32_bf16 v[144:147], a[144:147], v[16:19], v[144:147]\n\t"
    "v_exp_f32 v4, v4\n\t"
    "v_cvt_pk_bf16_f32 v93, v94, v95\n\t"
    "v_mfma_f32_16x16x32_bf16 v[148:151], a[148:151], v[16:19], v[148:151]\n\t"
    "v_exp_f32 v5, v5\n\t"
    "ds_write_b64 v58, v[92:93] offset:128\n\t"
    "v_mfma_f32_16x16x32_bf16 v[152:155], a[152:155], v[16:19], v[152:155]\n\t"
    "v_exp_f32 v6, v6\n\t"
    "ds_read_b128 v[64:67], v59 offset:0\n\t"
    "s_waitcnt lgkmcnt(2)\n\t"
    "v_mfma_f32_16x16x32_bf16 v[156:159], a[156:159], v[16:19], v[156:159]\n\t"
    "v_exp_f32 v7, v7\n\t"
    "ds_read_b128 v[68:71], v60 offset:1024\n\t"
    "v_mfma_f32_16x16x32_bf16 a[168:171], a[184:187], v[16:19], a[168:171]\n\t"
    "v_cvt_pk_bf16_f32 v16, v0, v1\n\t"
    "v_cvt_pk_bf16_f32 v17, v2, v3\n\t"
    "ds_read_b128 v[72:75], v59 offset:2048\n\t"
    "v_mfma_f32_16x16x32_bf16 v[160:163], a[128:131], v[20:23], v[160:163]\n\t"
    "v_cvt_pk_bf16_f32 v18, v4, v5\n\t"
    "v_cvt_pk_bf16_f32 v19, v6, v7\n\t"
    "ds_read_b128 v[76:79], v60 offset:3072\n\t"
    "v_mfma_f32_16x16x32_bf16 v[164:167], a[132:135], v[20:23], v[164:167]\n\t"
    "v_exp_f32 v8, v8\n\t"
    "v_mfma_f32_16x16x32_bf16 v[168:171], a[136:139], v[20:23], v[168:171]\n\t"
    "v_exp_f32 v9, v9\n\t"
    "v_mfma_f32_16x16x32_bf16 v[172:175], a[140:143], v[20:23], v[172:175]\n\t"
    "v_exp_f32 v10, v10\n\t"
    "v_mfma_f32_16x16x32_bf16 v[176:179], a[144:147], v[20:23], v[176:179]\n\t"
    "v_exp_f32 v11, v11\n\t"
    "v_accvgpr_read_b32 v63, a164\n\t"
    "v_mfma_f32_16x16x32_bf16 v[180:183], a[148:151], v[20:23], v[180:183]\n\t"
    "ds_read_b64_tr_b16 a[128:129], v50 offset:8192\n\t"
    "ds_read_b64_tr_b16 a[130:131], v50 offset:12288\n\t"
    "v_rcp_f32 v52, v63\n\t"
    "v_mfma_f32_16x16x32_bf16 v[184:187], a[152:155], v[20:23], v[184:187]\n\t"
    "ds_read_b64_tr_b16 a[132:133], v50 offset:8448\n\t"
    "ds_read_b64_tr_b16 a[134:135], v50 offset:12544\n\t"
    "v_cmp_nle_f32_e64 s[62:63], v63, s72\n\t"
    "v_mfma_f32_16x16x32_bf16 v[188:191], a[156:159], v[20:23], v[188:191]\n\t"
    "ds_read_b64_tr_b16 a[136:137], v50 offset:8704\n\t"
    "ds_read_b64_tr_b16 a[138:139], v50 offset:12800\n\t"
    "s_waitcnt lgkmcnt(6)\n\t"
    "buffer_store_dwordx4 v[64:67], v54, s[48:51], s75 offen nt\n\t"
    "v_mul_f32 v96, v96, v52\n\t"
    "v_mfma_f32_16x16x32_bf16 a[172:175], a[184:187], v[20:23], a[172:175]\n\t"
    "ds_read_b64_tr_b16 a[140:141], v50 offset:8960\n\t"
    "v_cvt_pk_bf16_f32 v20, v8, v9\n\t"
    "v_mul_f32 v97, v97, v52\n\t"
    "v_mfma_f32_16x16x32_bf16 v[0:3], a[96:99], a[32:35], v[32:35]\n\t"
    "ds_read_b64_tr_b16 a[142:143], v50 offset:13056\n\t"
    "v_cvt_pk_bf16_f32 v21, v10, v11\n\t"
    "v_mul_f32 v98, v98, v52\n\t"
    "v_mfma_f32_16x16x32_bf16 v[0:3], a[100:103], a[36:39], v[0:3]\n\t"
    "v_exp_f32 v12, v12\n\t"
    "v_mul_f32 v99, v99, v52\n\t"
    "v_mfma_f32_16x16x32_bf16 v[0:3], a[104:107], a[40:43], v[0:3]\n\t"
    "v_exp_f32 v13, v13\n\t"
    "v_cvt_pk_bf16_f32 v96, v96, v97\n\t"
    "v_cvt_pk_bf16_f32 v97, v98, v99\n\t"
    "v_mfma_f32_16x16x32_bf16 v[0:3], a[108:111], a[44:47], v[0:3]\n\t"
    "v_exp_f32 v14, v14\n\t"
    "ds_write_b64 v55, v[96:97] offset:4096\n\t"
    "v_mfma_f32_16x16x32_bf16 v[4:7], a[112:115], a[32:35], v[32:35]\n\t"
    "v_exp_f32 v15, v15\n\t"
    "v_mul_f32 v100, v100, v52\n\t"
    "v_mfma_f32_16x16x32_bf16 v[4:7], a[116:119], a[36:39], v[4:7]\n\t"
    "ds_read_b64_tr_b16 a[144:145], v50 offset:9216\n\t"
    "v_cvt_pk_bf16_f32 v22, v12, v13\n\t"
    "v_mul_f32 v101, v101, v52\n\t"
    "v_mfma_f32_16x16x32_bf16 v[4:7], a[120:123], a[40:43], v[4:7]\n\t"
    "ds_read_b64_tr_b16 a[146:147], v50 offset:13312\n\t"
    "v_cvt_pk_bf16_f32 v23, v14, v15\n\t"
    "v_mul_f32 v102, v102, v52\n\t"
    "v_mfma_f32_16x16x32_bf16 v[4:7], a[124:127], a[44:47], v[4:7]\n\t"
    "ds_read_b64_tr_b16 a[148:149], v50 offset:9472\n\t"
    "ds_read_b64_tr_b16 a[150:151], v50 offset:13568\n\t"
    "v_mul_f32 v103, v103, v52\n\t"
    "v_mfma_f32_16x16x32_bf16 v[8:11], a[96:99], a[48:51], v[36:39]\n\t"
    "ds_read_b64_tr_b16 a[152:153], v50 offset:9728\n\t"
    "ds_read_b64_tr_b16 a[154:155], v50 offset:13824\n\t"
    "buffer_store_dwordx4 v[68:71], v54, s[48:51], s75 offen offset:1024 nt\n\t"
    "v_cvt_pk_bf16_f32 v100, v100, v101\n\t"
    "v_mfma_f32_16x16x32_bf16 v[8:11], a[100:103], a[52:55], v[8:11]\n\t"
    "ds_read_b64_tr_b16 a[156:157], v50 offset:9984\n\t"
    "ds_read_b64_tr_b16 a[158:159], v50 offset:14080\n\t"
    "v_cvt_pk_bf16_f32 v101, v102, v103\n\t"
    "ds_write_b64 v56, v[100:101] offset:4096\n\t"
    "v_mfma_f32_16x16x32_bf16 v[8:11], a[104:107], a[56:59], v[8:11]\n\t"
    "ds_read_b128 a[96:99], v48 offset:24576\n\t"
    "ds_read_b128 a[100:103], v49 offset:24576\n\t"
    "v_mul_f32 v104, v104, v52\n\t"
    "v_mfma_f32_16x16x32_bf16 v[8:11], a[108:111], a[60:63], v[8:11]\n\t"
    "ds_read_b128 a[104:107], v48 offset:25600\n\t"
    "ds_read_b128 a[108:111], v49 offset:25600\n\t"
    "v_mul_f32 v105, v105, v52\n\t"
    "v_mfma_f32_16x16x32_bf16 v[12:15], a[112:115], a[48:51], v[36:39]\n\t"
    "ds_read_b128 a[112:115], v48 offset:28672\n\t"
    "v_mul_f32 v106, v106, v52\n\t"
    "v_mfma_f32_16x16x32_bf16 v[12:15], a[116:119], a[52:55], v[12:15]\n\t"
    "ds_read_b128 a[116:119], v49 offset:28672\n\t"
    "v_mul_f32 v107, v107, v52\n\t"
    "v_mfma_f32_16x16x32_bf16 v[12:15], a[120:123], a[56:59], v[12:15]\n\t"
    "ds_read_b128 a[120:123], v48 offset:29696\n\t"
    "v_cvt_pk_bf16_f32 v104, v104, v105\n\t"
    "v_mfma_f32_16x16x32_bf16 v[12:15], a[124:127], a[60:63], v[12:15]\n\t"
    "ds_read_b128 a[124:127], v49 offset:29696\n\t"
    "v_cvt_pk_bf16_f32 v105, v106, v107\n\t"
    "s_waitcnt lgkmcnt(11)\n\t"
    "v_mfma_f32_16x16x32_bf16 v[128:131], a[128:131], v[16:19], v[128:131]\n\t"
    "v_exp_f32 v0, v0\n\t"
    "ds_write_b64 v57, v[104:105] offset:4096\n\t"
    "v_mfma_f32_16x16x32_bf16 v[132:135], a[132:135], v[16:19], v[132:135]\n\t"
    "v_exp_f32 v1, v1\n\t"
    "v_mul_f32 v108, v108, v52\n\t"
    "v_mul_f32 v109, v109, v52\n\t"
    "v_mfma_f32_16x16x32_bf16 v[136:139], a[136:139], v[16:19], v[136:139]\n\t"
    "v_exp_f32 v2, v2\n\t"
    "buffer_store_dwordx4 v[72:75], v54, s[48:51], s75 offen offset:2048 nt\n\t"
    "v_mul_f32 v110, v110, v52\n\t"
    "v_mfma_f32_16x16x32_bf16 v[140:143], a[140:143], v[16:19], v[140:143]\n\t"
    "v_exp_f32 v3, v3\n\t"
    "v_mul_f32 v111, v111, v52\n\t"
    "v_mfma_f32_16x16x32_bf16 v[144:147], a[144:147], v[16:19], v[144:147]\n\t"
    "v_exp_f32 v4, v4\n\t"
    "v_cvt_pk_bf16_f32 v108, v108, v109\n\t"
    "v_mfma_f32_16x16x32_bf16 v[148:151], a[148:151], v[16:19], v[148:151]\n\t"
    "v_exp_f32 v5, v5\n\t"
    "v_cvt_pk_bf16_f32 v109, v110, v111\n\t"
    "v_mfma_f32_16x16x32_bf16 v[152:155], a[152:155], v[16:19], v[152:155]\n\t"
    "v_exp_f32 v6, v6\n\t"
    "ds_write_b64 v58, v[108:109] offset:4096\n\t"
    "s_waitcnt lgkmcnt(1)\n\t"
    "v_mfma_f32_16x16x32_bf16 v[156:159], a[156:159], v[16:19], v[156:159]\n\t"
    "v_exp_f32 v7, v7\n\t"
    "v_mul_f32 v112, v112, v52\n\t"
    "v_mfma_f32_16x16x32_bf16 a[168:171], a[184:187], v[16:19], a[168:171]\n\t"
    "v_cvt_pk_bf16_f32 v16, v0, v1\n\t"
    "v_cvt_pk_bf16_f32 v17, v2, v3\n\t"
    "v_mul_f32 v113, v113, v52\n\t"
    "v_mul_f32 v114, v114, v52\n\t"
    "v_mfma_f32_16x16x32_bf16 v[160:163], a[128:131], v[20:23], v[160:163]\n\t"
    "v_cvt_pk_bf16_f32 v18, v4, v5\n\t"
    "v_cvt_pk_bf16_f32 v19, v6, v7\n\t"
    "v_mul_f32 v115, v115, v52\n\t"
    "v_mfma_f32_16x16x32_bf16 v[164:167], a[132:135], v[20:23], v[164:167]\n\t"
    "v_exp_f32 v8, v8\n\t"
    "v_cvt_pk_bf16_f32 v112, v112, v113\n\t"
    "v_mfma_f32_16x16x32_bf16 v[168:171], a[136:139], v[20:23], v[168:171]\n\t"
    "v_exp_f32 v9, v9\n\t"
    "v_cvt_pk_bf16_f32 v113, v114, v115\n\t"
    "v_mfma_f32_16x16x32_bf16 v[172:175], a[140:143], v[20:23], v[172:175]\n\t"
    "v_exp_f32 v10, v10\n\t"
    "buffer_store_dwordx4 v[76:79], v54, s[48:51], s75 offen offset:3072 nt\n\t"
    "ds_write_b64 v55, v[112:113] offset:4224\n\t"
    "v_mfma_f32_16x16x32_bf16 v[176:179], a[144:147], v[20:23], v[176:179]\n\t"
    "v_exp_f32 v11, v11\n\t"
    "v_mul_f32 v116, v116, v52\n\t"
    "v_mfma_f32_16x16x32_bf16 v[180:183], a[148:151], v[20:23], v[180:183]\n\t"
    "ds_read_b64_tr_b16 a[128:129], v50 offset:16384\n\t"
    "ds_read_b64_tr_b16 a[130:131], v50 offset:20480\n\t"
    "v_mul_f32 v117, v117, v52\n\t"
    "v_mfma_f32_16x16x32_bf16 v[184:187], a[152:155], v[20:23], v[184:187]\n\t"
    "ds_read_b64_tr_b16 a[132:133], v50 offset:16640\n\t"
    "ds_read_b64_tr_b16 a[134:135], v50 offset:20736\n\t"
    "v_mul_f32 v118, v118, v52\n\t"
    "v_mfma_f32_16x16x32_bf16 v[188:191], a[156:159], v[20:23], v[188:191]\n\t"
    "ds_read_b64_tr_b16 a[136:137], v50 offset:16896\n\t"
    "ds_read_b64_tr_b16 a[138:139], v50 offset:20992\n\t"
    "v_mul_f32 v119, v119, v52\n\t"
    "v_cvt_pk_bf16_f32 v116, v116, v117\n\t"
    "v_mfma_f32_16x16x32_bf16 a[172:175], a[184:187], v[20:23], a[172:175]\n\t"
    "ds_read_b64_tr_b16 a[140:141], v50 offset:17152\n\t"
    "v_cvt_pk_bf16_f32 v20, v8, v9\n\t"
    "v_cvt_pk_bf16_f32 v117, v118, v119\n\t"
    "v_mfma_f32_16x16x32_bf16 v[0:3], a[96:99], a[32:35], v[32:35]\n\t"
    "ds_read_b64_tr_b16 a[142:143], v50 offset:21248\n\t"
    "v_cvt_pk_bf16_f32 v21, v10, v11\n\t"
    "ds_write_b64 v56, v[116:117] offset:4224\n\t"
    "v_mfma_f32_16x16x32_bf16 v[0:3], a[100:103], a[36:39], v[0:3]\n\t"
    "v_exp_f32 v12, v12\n\t"
    "v_mul_f32 v120, v120, v52\n\t"
    "v_mfma_f32_16x16x32_bf16 v[0:3], a[104:107], a[40:43], v[0:3]\n\t"
    "v_exp_f32 v13, v13\n\t"
    "v_mul_f32 v121, v121, v52\n\t"
    "v_mfma_f32_16x16x32_bf16 v[0:3], a[108:111], a[44:47], v[0:3]\n\t"
    "v_exp_f32 v14, v14\n\t"
    "v_mul_f32 v122, v122, v52\n\t"
    "v_mfma_f32_16x16x32_bf16 v[4:7], a[112:115], a[32:35], v[32:35]\n\t"
    "v_exp_f32 v15, v15\n\t"
    "v_mul_f32 v123, v123, v52\n\t"
    "v_mfma_f32_16x16x32_bf16 v[4:7], a[116:119], a[36:39], v[4:7]\n\t"
    "ds_read_b64_tr_b16 a[144:145], v50 offset:17408\n\t"
    "v_cvt_pk_bf16_f32 v22, v12, v13\n\t"
    "v_cvt_pk_bf16_f32 v120, v120, v121\n\t"
    "v_cvt_pk_bf16_f32 v121, v122, v123\n\t"
    "v_mfma_f32_16x16x32_bf16 v[4:7], a[120:123], a[40:43], v[4:7]\n\t"
    "ds_read_b64_tr_b16 a[146:147], v50 offset:21504\n\t"
    "v_cvt_pk_bf16_f32 v23, v14, v15\n\t"
    "ds_write_b64 v57, v[120:121] offset:4224\n\t"
    "v_mfma_f32_16x16x32_bf16 v[4:7], a[124:127], a[44:47], v[4:7]\n\t"
    "ds_read_b64_tr_b16 a[148:149], v50 offset:17664\n\t"
    "ds_read_b64_tr_b16 a[150:151], v50 offset:21760\n\t"
    "v_mul_f32 v124, v124, v52\n\t"
    "v_mfma_f32_16x16x32_bf16 v[8:11], a[96:99], a[48:51], v[36:39]\n\t"
    "ds_read_b64_tr_b16 a[152:153], v50 offset:17920\n\t"
    "ds_read_b64_tr_b16 a[154:155], v50 offset:22016\n\t"
    "v_mul_f32 v125, v125, v52\n\t"
    "v_mfma_f32_16x16x32_bf16 v[8:11], a[100:103], a[52:55], v[8:11]\n\t"
    "ds_read_b64_tr_b16 a[156:157], v50 offset:18176\n\t"
    "ds_read_b64_tr_b16 a[158:159], v50 offset:22272\n\t"
    "v_mul_f32 v126, v126, v52\n\t"
    "v_mfma_f32_16x16x32_bf16 v[8:11], a[104:107], a[56:59], v[8:11]\n\t"
    "ds_read_b128 a[96:99], v48 offset:57344\n\t"
    "ds_read_b128 a[100:103], v49 offset:57344\n\t"
    "v_mul_f32 v127, v127, v52\n\t"
    "v_mfma_f32_16x16x32_bf16 v[8:11], a[108:111], a[60:63], v[8:11]\n\t"
    "ds_read_b128 a[104:107], v48 offset:58368\n\t"
    "ds_read_b128 a[108:111], v49 offset:58368\n\t"
    "v_cvt_pk_bf16_f32 v124, v124, v125\n\t"
    "v_mfma_f32_16x16x32_bf16 v[12:15], a[112:115], a[48:51], v[36:39]\n\t"
    "ds_read_b128 a[112:115], v48 offset:61440\n\t"
    "v_cvt_pk_bf16_f32 v125, v126, v127\n\t"
    "v_mfma_f32_16x16x32_bf16 v[12:15], a[116:119], a[52:55], v[12:15]\n\t"
    "ds_read_b128 a[116:119], v49 offset:61440\n\t"
    "ds_write_b64 v58, v[124:125] offset:4224\n\t"
    "v_mfma_f32_16x16x32_bf16 v[12:15], a[120:123], a[56:59], v[12:15]\n\t"
    "ds_read_b128 a[120:123], v48 offset:62464\n\t"
    "ds_read_b128 v[96:99], v59 offset:4096\n\t"
    "v_mfma_f32_16x16x32_bf16 v[12:15], a[124:127], a[60:63], v[12:15]\n\t"
    "ds_read_b128 a[124:127], v49 offset:62464\n\t"
    "ds_read_b128 v[100:103], v60 offset:5120\n\t"
    "s_waitcnt lgkmcnt(13)\n\t"
    "v_mfma_f32_16x16x32_bf16 v[128:131], a[128:131], v[16:19], v[128:131]\n\t"
    "v_exp_f32 v0, v0\n\t"
    "ds_read_b128 v[104:107], v59 offset:6144\n\t"
    "v_mfma_f32_16x16x32_bf16 v[132:135], a[132:135], v[16:19], v[132:135]\n\t"
    "v_exp_f32 v1, v1\n\t"
    "ds_read_b128 v[108:111], v60 offset:7168\n\t"
    "v_mfma_f32_16x16x32_bf16 v[136:139], a[136:139], v[16:19], v[136:139]\n\t"
    "v_exp_f32 v2, v2\n\t"
    "v_mfma_f32_16x16x32_bf16 v[140:143], a[140:143], v[16:19], v[140:143]\n\t"
    "v_exp_f32 v3, v3\n\t"
    "v_mfma_f32_16x16x32_bf16 v[144:147], a[144:147], v[16:19], v[144:147]\n\t"
    "v_exp_f32 v4, v4\n\t"
    "v_mfma_f32_16x16x32_bf16 v[148:151], a[148:151], v[16:19], v[148:151]\n\t"
    "v_exp_f32 v5, v5\n\t"
    "v_mfma_f32_16x16x32_bf16 v[152:155], a[152:155], v[16:19], v[152:155]\n\t"
    "v_exp_f32 v6, v6\n\t"
    "s_waitcnt lgkmcnt(3)\n\t"
    "v_mfma_f32_16x16x32_bf16 v[156:159], a[156:159], v[16:19], v[156:159]\n\t"
    "v_exp_f32 v7, v7\n\t"
    "v_mfma_f32_16x16x32_bf16 a[168:171], a[184:187], v[16:19], a[168:171]\n\t"
    "v_cvt_pk_bf16_f32 v16, v0, v1\n\t"
    "v_cvt_pk_bf16_f32 v17, v2, v3\n\t"
    "buffer_store_dwordx4 v[96:99], v54, s[48:51], s31 offen nt\n\t"
    "v_mfma_f32_16x16x32_bf16 v[160:163], a[128:131], v[20:23], v[160:163]\n\t"
    "v_cvt_pk_bf16_f32 v18, v4, v5\n\t"
    "v_cvt_pk_bf16_f32 v19, v6, v7\n\t"
    "v_mfma_f32_16x16x32_bf16 v[164:167], a[132:135], v[20:23], v[164:167]\n\t"
    "v_exp_f32 v8, v8\n\t"
    "v_mfma_f32_16x16x32_bf16 v[168:171], a[136:139], v[20:23], v[168:171]\n\t"
    "v_exp_f32 v9, v9\n\t"
    "v_mfma_f32_16x16x32_bf16 v[172:175], a[140:143], v[20:23], v[172:175]\n\t"
    "v_exp_f32 v10, v10\n\t"
    "v_mfma_f32_16x16x32_bf16 v[176:179], a[144:147], v[20:23], v[176:179]\n\t"
    "v_exp_f32 v11, v11\n\t"
    "v_mfma_f32_16x16x32_bf16 v[180:183], a[148:151], v[20:23], v[180:183]\n\t"
    "ds_read_b64_tr_b16 a[128:129], v50 offset:24576\n\t"
    "ds_read_b64_tr_b16 a[130:131], v50 offset:28672\n\t"
    "v_mfma_f32_16x16x32_bf16 v[184:187], a[152:155], v[20:23], v[184:187]\n\t"
    "ds_read_b64_tr_b16 a[132:133], v50 offset:24832\n\t"
    "ds_read_b64_tr_b16 a[134:135], v50 offset:28928\n\t"
    "v_mfma_f32_16x16x32_bf16 v[188:191], a[156:159], v[20:23], v[188:191]\n\t"
    "ds_read_b64_tr_b16 a[136:137], v50 offset:25088\n\t"
    "ds_read_b64_tr_b16 a[138:139], v50 offset:29184\n\t"
    "v_mfma_f32_16x16x32_bf16 a[172:175], a[184:187], v[20:23], a[172:175]\n\t"
    "ds_read_b64_tr_b16 a[140:141], v50 offset:25344\n\t"
    "v_cvt_pk_bf16_f32 v20, v8, v9\n\t"
    "v_mfma_f32_16x16x32_bf16 v[0:3], a[96:99], a[64:67], v[40:43]\n\t"
    "ds_read_b64_tr_b16 a[142:143], v50 offset:29440\n\t"
    "v_cvt_pk_bf16_f32 v21, v10, v11\n\t"
    "s_waitcnt lgkmcnt(8)\n\t"
    "buffer_store_dwordx4 v[100:103], v54, s[48:51], s31 offen offset:1024 nt\n\t"
    "v_mfma_f32_16x16x32_bf16 v[0:3], a[100:103], a[68:71], v[0:3]\n\t"
    "v_exp_f32 v12, v12\n\t"
    "v_mfma_f32_16x16x32_bf16 v[0:3], a[104:107], a[72:75], v[0:3]\n\t"
    "v_exp_f32 v13, v13\n\t"
    "v_mfma_f32_16x16x32_bf16 v[0:3], a[108:111], a[76:79], v[0:3]\n\t"
    "v_exp_f32 v14, v14\n\t"
    "v_mfma_f32_16x16x32_bf16 v[4:7], a[112:115], a[64:67], v[40:43]\n\t"
    "v_exp_f32 v15, v15\n\t"
    "v_mfma_f32_16x16x32_bf16 v[4:7], a[116:119], a[68:71], v[4:7]\n\t"
    "ds_read_b64_tr_b16 a[144:145], v50 offset:25600\n\t"
    "v_cvt_pk_bf16_f32 v22, v12, v13\n\t"
    "v_mfma_f32_16x16x32_bf16 v[4:7], a[120:123], a[72:75], v[4:7]\n\t"
    "ds_read_b64_tr_b16 a[146:147], v50 offset:29696\n\t"
    "v_cvt_pk_bf16_f32 v23, v14, v15\n\t"
    "v_mfma_f32_16x16x32_bf16 v[4:7], a[124:127], a[76:79], v[4:7]\n\t"
    "ds_read_b64_tr_b16 a[148:149], v50 offset:25856\n\t"
    "ds_read_b64_tr_b16 a[150:151], v50 offset:29952\n\t"
    "v_mfma_f32_16x16x32_bf16 v[8:11], a[96:99], a[80:83], v[44:47]\n\t"
    "ds_read_b64_tr_b16 a[152:153], v50 offset:26112\n\t"
    "ds_read_b64_tr_b16 a[154:155], v50 offset:30208\n\t"
    "v_mfma_f32_16x16x32_bf16 v[8:11], a[100:103], a[84:87], v[8:11]\n\t"
    "ds_read_b64_tr_b16 a[156:157], v50 offset:26368\n\t"
    "ds_read_b64_tr_b16 a[158:159], v50 offset:30464\n\t"
    "v_mfma_f32_16x16x32_bf16 v[8:11], a[104:107], a[88:91], v[8:11]\n\t"
    "ds_read_b128 a[96:99], v48 offset:0\n\t"
    "ds_read_b128 a[100:103], v49 offset:0\n\t"
    "buffer_store_dwordx4 v[104:107], v54, s[48:51], s31 offen offset:2048 nt\n\t"
    "v_mfma_f32_16x16x32_bf16 v[8:11], a[108:111], a[92:95], v[8:11]\n\t"
    "ds_read_b128 a[104:107], v48 offset:1024\n\t"
    "ds_read_b128 a[108:111], v49 offset:1024\n\t"
    "v_mfma_f32_16x16x32_bf16 v[12:15], a[112:115], a[80:83], v[44:47]\n\t"
    "ds_read_b128 a[112:115], v48 offset:4096\n\t"
    "s_add_u32 s75, s55, 8192\n\t"
    "v_mfma_f32_16x16x32_bf16 v[12:15], a[116:119], a[84:87], v[12:15]\n\t"
    "ds_read_b128 a[116:119], v49 offset:4096\n\t"
    "v_mfma_f32_16x16x32_bf16 v[12:15], a[120:123], a[88:91], v[12:15]\n\t"
    "ds_read_b128 a[120:123], v48 offset:5120\n\t"
    "v_mfma_f32_16x16x32_bf16 v[12:15], a[124:127], a[92:95], v[12:15]\n\t"
    "ds_read_b128 a[124:127], v49 offset:5120\n\t"
    "s_waitcnt lgkmcnt(12)\n\t"
    "v_mfma_f32_16x16x32_bf16 v[128:131], a[128:131], v[16:19], v[128:131]\n\t"
    "v_exp_f32 v0, v0\n\t"
    "v_mfma_f32_16x16x32_bf16 v[132:135], a[132:135], v[16:19], v[132:135]\n\t"
    "v_exp_f32 v1, v1\n\t"
    "v_mfma_f32_16x16x32_bf16 v[136:139], a[136:139], v[16:19], v[136:139]\n\t"
    "v_exp_f32 v2, v2\n\t"
    "v_mfma_f32_16x16x32_bf16 v[140:143], a[140:143], v[16:19], v[140:143]\n\t"
    "v_exp_f32 v3, v3\n\t"
    "v_mfma_f32_16x16x32_bf16 v[144:147], a[144:147], v[16:19], v[144:147]\n\t"
    "v_exp_f32 v4, v4\n\t"
    "buffer_store_dwordx4 v[108:111], v54, s[48:51], s31 offen offset:3072 nt\n\t"
    "v_mfma_f32_16x16x32_bf16 v[148:151], a[148:151], v[16:19], v[148:151]\n\t"
    "v_exp_f32 v5, v5\n\t"
    "s_waitcnt lgkmcnt(2)\n\t"
    "v_mfma_f32_16x16x32_bf16 v[152:155], a[152:155], v[16:19], v[152:155]\n\t"
    "v_exp_f32 v6, v6\n\t"
    "v_mfma_f32_16x16x32_bf16 v[156:159], a[156:159], v[16:19], v[156:159]\n\t"
    "v_exp_f32 v7, v7\n\t"
    "v_mfma_f32_16x16x32_bf16 a[168:171], a[184:187], v[16:19], a[168:171]\n\t"
    "v_cvt_pk_bf16_f32 v16, v0, v1\n\t"
    "v_cvt_pk_bf16_f32 v17, v2, v3\n\t"
    "v_mfma_f32_16x16x32_bf16 v[160:163], a[128:131], v[20:23], v[160:163]\n\t"
    "v_cvt_pk_bf16_f32 v18, v4, v5\n\t"
    "v_cvt_pk_bf16_f32 v19, v6, v7\n\t"
    "v_mfma_f32_16x16x32_bf16 v[164:167], a[132:135], v[20:23], v[164:167]\n\t"
    "v_exp_f32 v8, v8\n\t"
    "v_mfma_f32_16x16x32_bf16 v[168:171], a[136:139], v[20:23], v[168:171]\n\t"
    "v_exp_f32 v9, v9\n\t"
    "v_mfma_f32_16x16x32_bf16 v[172:175], a[140:143], v[20:23], v[172:175]\n\t"
    "v_exp_f32 v10, v10\n\t"
    "v_mfma_f32_16x16x32_bf16 v[176:179], a[144:147], v[20:23], v[176:179]\n\t"
    "v_exp_f32 v11, v11\n\t"
    "v_mfma_f32_16x16x32_bf16 v[180:183], a[148:151], v[20:23], v[180:183]\n\t"
    "ds_read_b64_tr_b16 a[128:129], v50 offset:57344\n\t"
    "ds_read_b64_tr_b16 a[130:131], v50 offset:61440\n\t"
    "v_mfma_f32_16x16x32_bf16 v[184:187], a[152:155], v[20:23], v[184:187]\n\t"
    "ds_read_b64_tr_b16 a[132:133], v50 offset:57600\n\t"
    "ds_read_b64_tr_b16 a[134:135], v50 offset:61696\n\t"
    "v_mfma_f32_16x16x32_bf16 v[188:191], a[156:159], v[20:23], v[188:191]\n\t"
    "ds_read_b64_tr_b16 a[136:137], v50 offset:57856\n\t"
    "ds_read_b64_tr_b16 a[138:139], v50 offset:61952\n\t"
    "v_mfma_f32_16x16x32_bf16 a[172:175], a[184:187], v[20:23], a[172:175]\n\t"
    "ds_read_b64_tr_b16 a[140:141], v50 offset:58112\n\t"
    "v_cvt_pk_bf16_f32 v20, v8, v9\n\t"
    "v_mfma_f32_16x16x32_bf16 v[0:3], a[96:99], a[64:67], v[40:43]\n\t"
    "ds_read_b64_tr_b16 a[142:143], v50 offset:62208\n\t"
    "v_cvt_pk_bf16_f32 v21, v10, v11\n\t"
    "v_mfma_f32_16x16x32_bf16 v[0:3], a[100:103], a[68:71], v[0:3]\n\t"
    "v_exp_f32 v12, v12\n\t"
    "v_mfma_f32_16x16x32_bf16 v[0:3], a[104:107], a[72:75], v[0:3]\n\t"
    "v_exp_f32 v13, v13\n\t"
    "v_mfma_f32_16x16x32_bf16 v[0:3], a[108:111], a[76:79], v[0:3]\n\t"
    "v_exp_f32 v14, v14\n\t"
    "v_accvgpr_read_b32 v61, a168\n\t"
    "v_mfma_f32_16x16x32_bf16 v[4:7], a[112:115], a[64:67], v[40:43]\n\t"
    "v_exp_f32 v15, v15\n\t"
    "v_rcp_f32 v62, v61\n\t"
    "v_mfma_f32_16x16x32_bf16 v[4:7], a[116:119], a[68:71], v[4:7]\n\t"
    "ds_read_b64_tr_b16 a[144:145], v50 offset:58368\n\t"
    "v_cvt_pk_bf16_f32 v22, v12, v13\n\t"
    "v_cmp_nle_f32_e64 s[64:65], v61, s72\n\t"
    "s_waitcnt lgkmcnt(3)\n\t"
    "v_mfma_f32_16x16x32_bf16 v[4:7], a[120:123], a[72:75], v[4:7]\n\t"
    "ds_read_b64_tr_b16 a[146:147], v50 offset:62464\n\t"
    "v_cvt_pk_bf16_f32 v23, v14, v15\n\t"
    "v_mul_f32 v128, v128, v62\n\t"
    "v_mfma_f32_16x16x32_bf16 v[4:7], a[124:127], a[76:79], v[4:7]\n\t"
    "ds_read_b64_tr_b16 a[148:149], v50 offset:58624\n\t"
    "ds_read_b64_tr_b16 a[150:151], v50 offset:62720\n\t"
    "v_mul_f32 v129, v129, v62\n\t"
    "v_mfma_f32_16x16x32_bf16 v[8:11], a[96:99], a[80:83], v[44:47]\n\t"
    "ds_read_b64_tr_b16 a[152:153], v50 offset:58880\n\t"
    "ds_read_b64_tr_b16 a[154:155], v50 offset:62976\n\t"
    "v_mul_f32 v130, v130, v62\n\t"
    "v_mfma_f32_16x16x32_bf16 v[8:11], a[100:103], a[84:87], v[8:11]\n\t"
    "ds_read_b64_tr_b16 a[156:157], v50 offset:59136\n\t"
    "ds_read_b64_tr_b16 a[158:159], v50 offset:63232\n\t"
    "v_mul_f32 v131, v131, v62\n\t"
    "v_mfma_f32_16x16x32_bf16 v[8:11], a[104:107], a[88:91], v[8:11]\n\t"
    "ds_read_b128 a[96:99], v48 offset:8192\n\t"
    "ds_read_b128 a[100:103], v49 offset:8192\n\t"
    "v_cvt_pk_bf16_f32 v128, v128, v129\n\t"
    "v_cvt_pk_bf16_f32 v129, v130, v131\n\t"
    "v_mfma_f32_16x16x32_bf16 v[8:11], a[108:111], a[92:95], v[8:11]\n\t"
    "ds_read_b128 a[104:107], v48 offset:9216\n\t"
    "ds_read_b128 a[108:111], v49 offset:9216\n\t"
    "ds_write_b64 v55, v[128:129] offset:0\n\t"
    "v_mfma_f32_16x16x32_bf16 v[12:15], a[112:115], a[80:83], v[44:47]\n\t"
    "ds_read_b128 a[112:115], v48 offset:12288\n\t"
    "v_mul_f32 v132, v132, v62\n\t"
    "v_mfma_f32_16x16x32_bf16 v[12:15], a[116:119], a[84:87], v[12:15]\n\t"
    "ds_read_b128 a[116:119], v49 offset:12288\n\t"
    "v_mul_f32 v133, v133, v62\n\t"
    "v_mfma_f32_16x16x32_bf16 v[12:15], a[120:123], a[88:91], v[12:15]\n\t"
    "ds_read_b128 a[120:123], v48 offset:13312\n\t"
    "v_mul_f32 v134, v134, v62\n\t"
    "v_mfma_f32_16x16x32_bf16 v[12:15], a[124:127], a[92:95], v[12:15]\n\t"
    "ds_read_b128 a[124:127], v49 offset:13312\n\t"
    "v_mul_f32 v135, v135, v62\n\t"
    "v_mfma_f32_16x16x32_bf16 v[192:195], a[128:131], v[16:19], v[192:195]\n\t"
    "v_exp_f32 v0, v0\n\t"
    "v_cvt_pk_bf16_f32 v132, v132, v133\n\t"
    "v_mfma_f32_16x16x32_bf16 v[196:199], a[132:135], v[16:19], v[196:199]\n\t"
    "v_exp_f32 v1, v1\n\t"
    "v_cvt_pk_bf16_f32 v133, v134, v135\n\t"
    "ds_write_b64 v56, v[132:133] offset:0\n\t"
    "v_mfma_f32_16x16x32_bf16 v[200:203], a[136:139], v[16:19], v[200:203]\n\t"
    "v_exp_f32 v2, v2\n\t"
    "v_mul_f32 v136, v136, v62\n\t"
    "s_waitcnt lgkmcnt(5)\n\t"
    "v_mfma_f32_16x16x32_bf16 v[204:207], a[140:143], v[16:19], v[204:207]\n\t"
    "v_exp_f32 v3, v3\n\t"
    "v_mul_f32 v137, v137, v62\n\t"
    "v_mfma_f32_16x16x32_bf16 v[208:211], a[144:147], v[16:19], v[208:211]\n\t"
    "v_exp_f32 v4, v4\n\t"
    "v_mul_f32 v138, v138, v62\n\t"
    "v_mfma_f32_16x16x32_bf16 v[212:215], a[148:151], v[16:19], v[212:215]\n\t"
    "v_exp_f32 v5, v5\n\t"
    "v_mul_f32 v139, v139, v62\n\t"
    "v_mfma_f32_16x16x32_bf16 v[216:219], a[152:155], v[16:19], v[216:219]\n\t"
    "v_exp_f32 v6, v6\n\t"
    "v_cvt_pk_bf16_f32 v136, v136, v137\n\t"
    "v_mfma_f32_16x16x32_bf16 v[220:223], a[156:159], v[16:19], v[220:223]\n\t"
    "v_exp_f32 v7, v7\n\t"
    "v_cvt_pk_bf16_f32 v137, v138, v139\n\t"
    "v_mfma_f32_16x16x32_bf16 a[176:179], a[184:187], v[16:19], a[176:179]\n\t"
    "v_cvt_pk_bf16_f32 v16, v0, v1\n\t"
    "v_cvt_pk_bf16_f32 v17, v2, v3\n\t"
    "ds_write_b64 v57, v[136:137] offset:0\n\t"
    "v_mfma_f32_16x16x32_bf16 v[224:227], a[128:131], v[20:23], v[224:227]\n\t"
    "v_cvt_pk_bf16_f32 v18, v4, v5\n\t"
    "v_cvt_pk_bf16_f32 v19, v6, v7\n\t"
    "v_mul_f32 v140, v140, v62\n\t"
    "v_mul_f32 v141, v141, v62\n\t"
    "v_mfma_f32_16x16x32_bf16 v[228:231], a[132:135], v[20:23], v[228:231]\n\t"
    "v_exp_f32 v8, v8\n\t"
    "v_mul_f32 v142, v142, v62\n\t"
    "v_mfma_f32_16x16x32_bf16 v[232:235], a[136:139], v[20:23], v[232:235]\n\t"
    "v_exp_f32 v9, v9\n\t"
    "v_mul_f32 v143, v143, v62\n\t"
    "v_mfma_f32_16x16x32_bf16 v[236:239], a[140:143], v[20:23], v[236:239]\n\t"
    "v_exp_f32 v10, v10\n\t"
    "v_cvt_pk_bf16_f32 v140, v140, v141\n\t"
    "v_mfma_f32_16x16x32_bf16 v[240:243], a[144:147], v[20:23], v[240:243]\n\t"
    "v_exp_f32 v11, v11\n\t"
    "v_cvt_pk_bf16_f32 v141, v142, v143\n\t"
    "v_mfma_f32_16x16x32_bf16 v[244:247], a[148:151], v[20:23], v[244:247]\n\t"
    "ds_read_b64_tr_b16 a[128:129], v50 offset:0\n\t"
    "ds_read_b64_tr_b16 a[130:131], v50 offset:4096\n\t"
    "ds_write_b64 v58, v[140:141] offset:0\n\t"
    "v_mfma_f32_16x16x32_bf16 v[248:251], a[152:155], v[20:23], v[248:251]\n\t"
    "ds_read_b64_tr_b16 a[132:133], v50 offset:256\n\t"
    "ds_read_b64_tr_b16 a[134:135], v50 offset:4352\n\t"
    "v_mul_f32 v144, v144, v62\n\t"
    "v_mfma_f32_16x16x32_bf16 v[252:255], a[156:159], v[20:23], v[252:255]\n\t"
    "ds_read_b64_tr_b16 a[136:137], v50 offset:512\n\t"
    "ds_read_b64_tr_b16 a[138:139], v50 offset:4608\n\t"
    "v_mul_f32 v145, v145, v62\n\t"
    "v_mul_f32 v146, v146, v62\n\t"
    "v_mfma_f32_16x16x32_bf16 a[180:183], a[184:187], v[20:23], a[180:183]\n\t"
    "ds_read_b64_tr_b16 a[140:141], v50 offset:768\n\t"
    "v_cvt_pk_bf16_f32 v20, v8, v9\n\t"
    "v_mul_f32 v147, v147, v62\n\t"
    "v_mfma_f32_16x16x32_bf16 v[0:3], a[96:99], a[64:67], v[40:43]\n\t"
    "ds_read_b64_tr_b16 a[142:143], v50 offset:4864\n\t"
    "v_cvt_pk_bf16_f32 v21, v10, v11\n\t"
    "v_cvt_pk_bf16_f32 v144, v144, v145\n\t"
    "v_mfma_f32_16x16x32_bf16 v[0:3], a[100:103], a[68:71], v[0:3]\n\t"
    "v_exp_f32 v12, v12\n\t"
    "v_cvt_pk_bf16_f32 v145, v146, v147\n\t"
    "v_mfma_f32_16x16x32_bf16 v[0:3], a[104:107], a[72:75], v[0:3]\n\t"
    "v_exp_f32 v13, v13\n\t"
    "ds_write_b64 v55, v[144:145] offset:128\n\t"
    "v_mfma_f32_16x16x32_bf16 v[0:3], a[108:111], a[76:79], v[0:3]\n\t"
    "v_exp_f32 v14, v14\n\t"
    "v_mul_f32 v148, v148, v62\n\t"
    "s_waitcnt lgkmcnt(5)\n\t"
    "v_mfma_f32_16x16x32_bf16 v[4:7], a[112:115], a[64:67], v[40:43]\n\t"
    "v_exp_f32 v15, v15\n\t"
    "v_mul_f32 v149, v149, v62\n\t"
    "v_mfma_f32_16x16x32_bf16 v[4:7], a[116:119], a[68:71], v[4:7]\n\t"
    "ds_read_b64_tr_b16 a[144:145], v50 offset:1024\n\t"
    "v_cvt_pk_bf16_f32 v22, v12, v13\n\t"
    "v_mul_f32 v150, v150, v62\n\t"
    "v_mfma_f32_16x16x32_bf16 v[4:7], a[120:123], a[72:75], v[4:7]\n\t"
    "ds_read_b64_tr_b16 a[146:147], v50 offset:5120\n\t"
    "v_cvt_pk_bf16_f32 v23, v14, v15\n\t"
    "v_mul_f32 v151, v151, v62\n\t"
    "v_cvt_pk_bf16_f32 v148, v148, v149\n\t"
    "v_mfma_f32_16x16x32_bf16 v[4:7], a[124:127], a[76:79], v[4:7]\n\t"
    "ds_read_b64_tr_b16 a[148:149], v50 offset:1280\n\t"
    "ds_read_b64_tr_b16 a[150:151], v50 offset:5376\n\t"
    "v_cvt_pk_bf16_f32 v149, v150, v151\n\t"
    "v_mfma_f32_16x16x32_bf16 v[8:11], a[96:99], a[80:83], v[44:47]\n\t"
    "ds_read_b64_tr_b16 a[152:153], v50 offset:1536\n\t"
    "ds_read_b64_tr_b16 a[154:155], v50 offset:5632\n\t"
    "ds_write_b64 v56, v[148:149] offset:128\n\t"
    "v_mfma_f32_16x16x32_bf16 v[8:11], a[100:103], a[84:87], v[8:11]\n\t"
    "ds_read_b64_tr_b16 a[156:157], v50 offset:1792\n\t"
    "ds_read_b64_tr_b16 a[158:159], v50 offset:5888\n\t"
    "v_mul_f32 v152, v152, v62\n\t"
    "v_mfma_f32_16x16x32_bf16 v[8:11], a[104:107], a[88:91], v[8:11]\n\t"
    "ds_read_b128 a[96:99], v48 offset:16384\n\t"
    "ds_read_b128 a[100:103], v49 offset:16384\n\t"
    "v_mul_f32 v153, v153, v62\n\t"
    "v_mfma_f32_16x16x32_bf16 v[8:11], a[108:111], a[92:95], v[8:11]\n\t"
    "ds_read_b128 a[104:107], v48 offset:17408\n\t"
    "ds_read_b128 a[108:111], v49 offset:17408\n\t"
    "v_mul_f32 v154, v154, v62\n\t"
    "v_mfma_f32_16x16x32_bf16 v[12:15], a[112:115], a[80:83], v[44:47]\n\t"
    "ds_read_b128 a[112:115], v48 offset:20480\n\t"
    "v_mul_f32 v155, v155, v62\n\t"
    "v_mfma_f32_16x16x32_bf16 v[12:15], a[116:119], a[84:87], v[12:15]\n\t"
    "ds_read_b128 a[116:119], v49 offset:20480\n\t"
    "v_cvt_pk_bf16_f32 v152, v152, v153\n\t"
    "v_cvt_pk_bf16_f32 v153, v154, v155\n\t"
    "v_mfma_f32_16x16x32_bf16 v[12:15], a[120:123], a[88:91], v[12:15]\n\t"
    "ds_read_b128 a[120:123], v48 offset:21504\n\t"
    "ds_write_b64 v57, v[152:153] offset:128\n\t"
    "v_mfma_f32_16x16x32_bf16 v[12:15], a[124:127], a[92:95], v[12:15]\n\t"
    "ds_read_b128 a[124:127], v49 offset:21504\n\t"
    "v_mul_f32 v156, v156, v62\n\t"
    "v_mfma_f32_16x16x32_bf16 v[192:195], a[128:131], v[16:19], v[192:195]\n\t"
    "v_exp_f32 v0, v0\n\t"
    "v_mul_f32 v157, v157, v62\n\t"
    "v_mfma_f32_16x16x32_bf16 v[196:199], a[132:135], v[16:19], v[196:199]\n\t"
    "v_exp_f32 v1, v1\n\t"
    "v_mul_f32 v158, v158, v62\n\t"
    "s_waitcnt lgkmcnt(7)\n\t"
    "v_mfma_f32_16x16x32_bf16 v[200:203], a[136:139], v[16:19], v[200:203]\n\t"
    "v_exp_f32 v2, v2\n\t"
    "v_mul_f32 v159, v159, v62\n\t"
    "v_mfma_f32_16x16x32_bf16 v[204:207], a[140:143], v[16:19], v[204:207]\n\t"
    "v_exp_f32 v3, v3\n\t"
    "v_cvt_pk_bf16_f32 v156, v156, v157\n\t"
    "v_mfma_f32_16x16x32_bf16 v[208:211], a[144:147], v[16:19], v[208:211]\n\t"
    "v_exp_f32 v4, v4\n\t"
    "v_cvt_pk_bf16_f32 v157, v158, v159\n\t"
    "v_mfma_f32_16x16x32_bf16 v[212:215], a[148:151], v[16:19], v[212:215]\n\t"
    "v_exp_f32 v5, v5\n\t"
    "ds_write_b64 v58, v[156:157] offset:128\n\t"
    "v_mfma_f32_16x16x32_bf16 v[216:219], a[152:155], v[16:19], v[216:219]\n\t"
    "v_exp_f32 v6, v6\n\t"
    "ds_read_b128 v[128:131], v59 offset:0\n\t"
    "v_mfma_f32_16x16x32_bf16 v[220:223], a[156:159], v[16:19], v[220:223]\n\t"
    "v_exp_f32 v7, v7\n\t"
    "ds_read_b128 v[132:135], v60 offset:1024\n\t"
    "v_mfma_f32_16x16x32_bf16 a[176:179], a[184:187], v[16:19], a[176:179]\n\t"
    "v_cvt_pk_bf16_f32 v16, v0, v1\n\t"
    "v_cvt_pk_bf16_f32 v17, v2, v3\n\t"
    "ds_read_b128 v[136:139], v59 offset:2048\n\t"
    "v_mfma_f32_16x16x32_bf16 v[224:227], a[128:131], v[20:23], v[224:227]\n\t"
    "v_cvt_pk_bf16_f32 v18, v4, v5\n\t"
    "v_cvt_pk_bf16_f32 v19, v6, v7\n\t"
    "ds_read_b128 v[140:143], v60 offset:3072\n\t"
    "v_mfma_f32_16x16x32_bf16 v[228:231], a[132:135], v[20:23], v[228:231]\n\t"
    "v_exp_f32 v8, v8\n\t"
    "v_mfma_f32_16x16x32_bf16 v[232:235], a[136:139], v[20:23], v[232:235]\n\t"
    "v_exp_f32 v9, v9\n\t"
    "v_mfma_f32_16x16x32_bf16 v[236:239], a[140:143], v[20:23], v[236:239]\n\t"
    "v_exp_f32 v10, v10\n\t"
    "v_mfma_f32_16x16x32_bf16 v[240:243], a[144:147], v[20:23], v[240:243]\n\t"
    "v_exp_f32 v11, v11\n\t"
    "v_accvgpr_read_b32 v63, a172\n\t"
    "v_mfma_f32_16x16x32_bf16 v[244:247], a[148:151], v[20:23], v[244:247]\n\t"
    "ds_read_b64_tr_b16 a[128:129], v50 offset:8192\n\t"
    "ds_read_b64_tr_b16 a[130:131], v50 offset:12288\n\t"
    "v_rcp_f32 v52, v63\n\t"
    "v_mfma_f32_16x16x32_bf16 v[248:251], a[152:155], v[20:23], v[248:251]\n\t"
    "ds_read_b64_tr_b16 a[132:133], v50 offset:8448\n\t"
    "ds_read_b64_tr_b16 a[134:135], v50 offset:12544\n\t"
    "v_cmp_nle_f32_e64 s[66:67], v63, s72\n\t"
    "v_mfma_f32_16x16x32_bf16 v[252:255], a[156:159], v[20:23], v[252:255]\n\t"
    "ds_read_b64_tr_b16 a[136:137], v50 offset:8704\n\t"
    "ds_read_b64_tr_b16 a[138:139], v50 offset:12800\n\t"
    "s_waitcnt lgkmcnt(6)\n\t"
    "buffer_store_dwordx4 v[128:131], v54, s[48:51], s75 offen nt\n\t"
    "v_mul_f32 v160, v160, v52\n\t"
    "v_mfma_f32_16x16x32_bf16 a[180:183], a[184:187], v[20:23], a[180:183]\n\t"
    "ds_read_b64_tr_b16 a[140:141], v50 offset:8960\n\t"
    "v_cvt_pk_bf16_f32 v20, v8, v9\n\t"
    "v_mul_f32 v161, v161, v52\n\t"
    "v_mfma_f32_16x16x32_bf16 v[0:3], a[96:99], a[64:67], v[40:43]\n\t"
    "ds_read_b64_tr_b16 a[142:143], v50 offset:13056\n\t"
    "v_cvt_pk_bf16_f32 v21, v10, v11\n\t"
    "v_mul_f32 v162, v162, v52\n\t"
    "v_mfma_f32_16x16x32_bf16 v[0:3], a[100:103], a[68:71], v[0:3]\n\t"
    "v_exp_f32 v12, v12\n\t"
    "v_mul_f32 v163, v163, v52\n\t"
    "v_mfma_f32_16x16x32_bf16 v[0:3], a[104:107], a[72:75], v[0:3]\n\t"
    "v_exp_f32 v13, v13\n\t"
    "v_cvt_pk_bf16_f32 v160, v160, v161\n\t"
    "v_cvt_pk_bf16_f32 v161, v162, v163\n\t"
    "v_mfma_f32_16x16x32_bf16 v[0:3], a[108:111], a[76:79], v[0:3]\n\t"
    "v_exp_f32 v14, v14\n\t"
    "ds_write_b64 v55, v[160:161] offset:4096\n\t"
    "v_mfma_f32_16x16x32_bf16 v[4:7], a[112:115], a[64:67], v[40:43]\n\t"
    "v_exp_f32 v15, v15\n\t"
    "v_mul_f32 v164, v164, v52\n\t"
    "v_mfma_f32_16x16x32_bf16 v[4:7], a[116:119], a[68:71], v[4:7]\n\t"
    "ds_read_b64_tr_b16 a[144:145], v50 offset:9216\n\t"
    "v_cvt_pk_bf16_f32 v22, v12, v13\n\t"
    "v_mul_f32 v165, v165, v52\n\t"
    "v_mfma_f32_16x16x32_bf16 v[4:7], a[120:123], a[72:75], v[4:7]\n\t"
    "ds_read_b64_tr_b16 a[146:147], v50 offset:13312\n\t"
    "v_cvt_pk_bf16_f32 v23, v14, v15\n\t"
    "v_mul_f32 v166, v166, v52\n\t"
    "v_mfma_f32_16x16x32_bf16 v[4:7], a[124:127], a[76:79], v[4:7]\n\t"
    "ds_read_b64_tr_b16 a[148:149], v50 offset:9472\n\t"
    "ds_read_b64_tr_b16 a[150:151], v50 offset:13568\n\t"
    "v_mul_f32 v167, v167, v52\n\t"
    "v_mfma_f32_16x16x32_bf16 v[8:11], a[96:99], a[80:83], v[44:47]\n\t"
    "ds_read_b64_tr_b16 a[152:153], v50 offset:9728\n\t"
    "ds_read_b64_tr_b16 a[154:155], v50 offset:13824\n\t"
    "buffer_store_dwordx4 v[132:135], v54, s[48:51], s75 offen offset:1024 nt\n\t"
    "v_cvt_pk_bf16_f32 v164, v164, v165\n\t"
    "v_mfma_f32_16x16x32_bf16 v[8:11], a[100:103], a[84:87], v[8:11]\n\t"
    "ds_read_b64_tr_b16 a[156:157], v50 offset:9984\n\t"
    "ds_read_b64_tr_b16 a[158:159], v50 offset:14080\n\t"
    "v_cvt_pk_bf16_f32 v165, v166, v167\n\t"
    "ds_write_b64 v56, v[164:165] offset:4096\n\t"
    "v_mfma_f32_16x16x32_bf16 v[8:11], a[104:107], a[88:91], v[8:11]\n\t"
    "ds_read_b128 a[96:99], v48 offset:24576\n\t"
    "ds_read_b128 a[100:103], v49 offset:24576\n\t"
    "v_mul_f32 v168, v168, v52\n\t"
    "v_mfma_f32_16x16x32_bf16 v[8:11], a[108:111], a[92:95], v[8:11]\n\t"
    "ds_read_b128 a[104:107], v48 offset:25600\n\t"
    "ds_read_b128 a[108:111], v49 offset:25600\n\t"
    "v_mul_f32 v169, v169, v52\n\t"
    "v_mfma_f32_16x16x32_bf16 v[12:15], a[112:115], a[80:83], v[44:47]\n\t"
    "ds_read_b128 a[112:115], v48 offset:28672\n\t"
    "v_mul_f32 v170, v170, v52\n\t"
    "v_mfma_f32_16x16x32_bf16 v[12:15], a[116:119], a[84:87], v[12:15]\n\t"
    "ds_read_b128 a[116:119], v49 offset:28672\n\t"
    "v_mul_f32 v171, v171, v52\n\t"
    "v_mfma_f32_16x16x32_bf16 v[12:15], a[120:123], a[88:91], v[12:15]\n\t"
    "ds_read_b128 a[120:123], v48 offset:29696\n\t"
    "v_cvt_pk_bf16_f32 v168, v168, v169\n\t"
    "v_mfma_f32_16x16x32_bf16 v[12:15], a[124:127], a[92:95], v[12:15]\n\t"
    "ds_read_b128 a[124:127], v49 offset:29696\n\t"
    "v_cvt_pk_bf16_f32 v169, v170, v171\n\t"
    "s_waitcnt lgkmcnt(11)\n\t"
    "v_mfma_f32_16x16x32_bf16 v[192:195], a[128:131], v[16:19], v[192:195]\n\t"
    "v_exp_f32 v0, v0\n\t"
    "ds_write_b64 v57, v[168:169] offset:4096\n\t"
    "v_mfma_f32_16x16x32_bf16 v[196:199], a[132:135], v[16:19], v[196:199]\n\t"
    "v_exp_f32 v1, v1\n\t"
    "v_mul_f32 v172, v172, v52\n\t"
    "v_mul_f32 v173, v173, v52\n\t"
    "v_mfma_f32_16x16x32_bf16 v[200:203], a[136:139], v[16:19], v[200:203]\n\t"
    "v_exp_f32 v2, v2\n\t"
    "buffer_store_dwordx4 v[136:139], v54, s[48:51], s75 offen offset:2048 nt\n\t"
    "v_mul_f32 v174, v174, v52\n\t"
    "v_mfma_f32_16x16x32_bf16 v[204:207], a[140:143], v[16:19], v[204:207]\n\t"
    "v_exp_f32 v3, v3\n\t"
    "v_mul_f32 v175, v175, v52\n\t"
    "v_mfma_f32_16x16x32_bf16 v[208:211], a[144:147], v[16:19], v[208:211]\n\t"
    "v_exp_f32 v4, v4\n\t"
    "v_cvt_pk_bf16_f32 v172, v172, v173\n\t"
    "v_mfma_f32_16x16x32_bf16 v[212:215], a[148:151], v[16:19], v[212:215]\n\t"
    "v_exp_f32 v5, v5\n\t"
    "v_cvt_pk_bf16_f32 v173, v174, v175\n\t"
    "v_mfma_f32_16x16x32_bf16 v[216:219], a[152:155], v[16:19], v[216:219]\n\t"
    "v_exp_f32 v6, v6\n\t"
    "ds_write_b64 v58, v[172:173] offset:4096\n\t"
    "s_waitcnt lgkmcnt(1)\n\t"
    "v_mfma_f32_16x16x32_bf16 v[220:223], a[156:159], v[16:19], v[220:223]\n\t"
    "v_exp_f32 v7, v7\n\t"
    "v_mul_f32 v176, v176, v52\n\t"
    "v_mfma_f32_16x16x32_bf16 a[176:179], a[184:187], v[16:19], a[176:179]\n\t"
    "v_cvt_pk_bf16_f32 v16, v0, v1\n\t"
    "v_cvt_pk_bf16_f32 v17, v2, v3\n\t"
    "v_mul_f32 v177, v177, v52\n\t"
    "v_mul_f32 v178, v178, v52\n\t"
    "v_mfma_f32_16x16x32_bf16 v[224:227], a[128:131], v[20:23], v[224:227]\n\t"
    "v_cvt_pk_bf16_f32 v18, v4, v5\n\t"
    "v_cvt_pk_bf16_f32 v19, v6, v7\n\t"
    "v_mul_f32 v179, v179, v52\n\t"
    "v_mfma_f32_16x16x32_bf16 v[228:231], a[132:135], v[20:23], v[228:231]\n\t"
    "v_exp_f32 v8, v8\n\t"
    "v_cvt_pk_bf16_f32 v176, v176, v177\n\t"
    "v_mfma_f32_16x16x32_bf16 v[232:235], a[136:139], v[20:23], v[232:235]\n\t"
    "v_exp_f32 v9, v9\n\t"
    "v_cvt_pk_bf16_f32 v177, v178, v179\n\t"
    "v_mfma_f32_16x16x32_bf16 v[236:239], a[140:143], v[20:23], v[236:239]\n\t"
    "v_exp_f32 v10, v10\n\t"
    "buffer_store_dwordx4 v[140:143], v54, s[48:51], s75 offen offset:3072 nt\n\t"
    "ds_write_b64 v55, v[176:177] offset:4224\n\t"
    "v_mfma_f32_16x16x32_bf16 v[240:243], a[144:147], v[20:23], v[240:243]\n\t"
    "v_exp_f32 v11, v11\n\t"
    "v_mul_f32 v180, v180, v52\n\t"
    "v_mfma_f32_16x16x32_bf16 v[244:247], a[148:151], v[20:23], v[244:247]\n\t"
    "ds_read_b64_tr_b16 a[128:129], v50 offset:16384\n\t"
    "ds_read_b64_tr_b16 a[130:131], v50 offset:20480\n\t"
    "v_mul_f32 v181, v181, v52\n\t"
    "v_mfma_f32_16x16x32_bf16 v[248:251], a[152:155], v[20:23], v[248:251]\n\t"
    "ds_read_b64_tr_b16 a[132:133], v50 offset:16640\n\t"
    "ds_read_b64_tr_b16 a[134:135], v50 offset:20736\n\t"
    "v_mul_f32 v182, v182, v52\n\t"
    "v_mfma_f32_16x16x32_bf16 v[252:255], a[156:159], v[20:23], v[252:255]\n\t"
    "ds_read_b64_tr_b16 a[136:137], v50 offset:16896\n\t"
    "ds_read_b64_tr_b16 a[138:139], v50 offset:20992\n\t"
    "v_mul_f32 v183, v183, v52\n\t"
    "v_cvt_pk_bf16_f32 v180, v180, v181\n\t"
    "v_mfma_f32_16x16x32_bf16 a[180:183], a[184:187], v[20:23], a[180:183]\n\t"
    "ds_read_b64_tr_b16 a[140:141], v50 offset:17152\n\t"
    "v_cvt_pk_bf16_f32 v20, v8, v9\n\t"
    "v_cvt_pk_bf16_f32 v181, v182, v183\n\t"
    "v_mfma_f32_16x16x32_bf16 v[0:3], a[96:99], a[64:67], v[40:43]\n\t"
    "ds_read_b64_tr_b16 a[142:143], v50 offset:21248\n\t"
    "v_cvt_pk_bf16_f32 v21, v10, v11\n\t"
    "ds_write_b64 v56, v[180:181] offset:4224\n\t"
    "v_mfma_f32_16x16x32_bf16 v[0:3], a[100:103], a[68:71], v[0:3]\n\t"
    "v_exp_f32 v12, v12\n\t"
    "v_mul_f32 v184, v184, v52\n\t"
    "v_mfma_f32_16x16x32_bf16 v[0:3], a[104:107], a[72:75], v[0:3]\n\t"
    "v_exp_f32 v13, v13\n\t"
    "v_mul_f32 v185, v185, v52\n\t"
    "v_mfma_f32_16x16x32_bf16 v[0:3], a[108:111], a[76:79], v[0:3]\n\t"
    "v_exp_f32 v14, v14\n\t"
    "v_mul_f32 v186, v186, v52\n\t"
    "v_mfma_f32_16x16x32_bf16 v[4:7], a[112:115], a[64:67], v[40:43]\n\t"
    "v_exp_f32 v15, v15\n\t"
    "v_mul_f32 v187, v187, v52\n\t"
    "v_mfma_f32_16x16x32_bf16 v[4:7], a[116:119], a[68:71], v[4:7]\n\t"
    "ds_read_b64_tr_b16 a[144:145], v50 offset:17408\n\t"
    "v_cvt_pk_bf16_f32 v22, v12, v13\n\t"
    "v_cvt_pk_bf16_f32 v184, v184, v185\n\t"
    "v_cvt_pk_bf16_f32 v185, v186, v187\n\t"
    "v_mfma_f32_16x16x32_bf16 v[4:7], a[120:123], a[72:75], v[4:7]\n\t"
    "ds_read_b64_tr_b16 a[146:147], v50 offset:21504\n\t"
    "v_cvt_pk_bf16_f32 v23, v14, v15\n\t"
    "ds_write_b64 v57, v[184:185] offset:4224\n\t"
    "v_mfma_f32_16x16x32_bf16 v[4:7], a[124:127], a[76:79], v[4:7]\n\t"
    "ds_read_b64_tr_b16 a[148:149], v50 offset:17664\n\t"
    "ds_read_b64_tr_b16 a[150:151], v50 offset:21760\n\t"
    "v_mul_f32 v188, v188, v52\n\t"
    "v_mfma_f32_16x16x32_bf16 v[8:11], a[96:99], a[80:83], v[44:47]\n\t"
    "ds_read_b64_tr_b16 a[152:153], v50 offset:17920\n\t"
    "ds_read_b64_tr_b16 a[154:155], v50 offset:22016\n\t"
    "v_mul_f32 v189, v189, v52\n\t"
    "v_mfma_f32_16x16x32_bf16 v[8:11], a[100:103], a[84:87], v[8:11]\n\t"
    "ds_read_b64_tr_b16 a[156:157], v50 offset:18176\n\t"
    "ds_read_b64_tr_b16 a[158:159], v50 offset:22272\n\t"
    "v_mul_f32 v190, v190, v52\n\t"
    "v_mfma_f32_16x16x32_bf16 v[8:11], a[104:107], a[88:91], v[8:11]\n\t"
    "v_exp_f32 v0, v0\n\t"
    "v_mul_f32 v191, v191, v52\n\t"
    "v_mfma_f32_16x16x32_bf16 v[8:11], a[108:111], a[92:95], v[8:11]\n\t"
    "v_exp_f32 v1, v1\n\t"
    "v_cvt_pk_bf16_f32 v188, v188, v189\n\t"
    "v_mfma_f32_16x16x32_bf16 v[12:15], a[112:115], a[80:83], v[44:47]\n\t"
    "v_exp_f32 v2, v2\n\t"
    "v_cvt_pk_bf16_f32 v189, v190, v191\n\t"
    "v_mfma_f32_16x16x32_bf16 v[12:15], a[116:119], a[84:87], v[12:15]\n\t"
    "v_exp_f32 v3, v3\n\t"
    "ds_write_b64 v58, v[188:189] offset:4224\n\t"
    "s_add_u32 s31, s55, 12288\n\t"
    "v_mfma_f32_16x16x32_bf16 v[12:15], a[120:123], a[88:91], v[12:15]\n\t"
    "v_exp_f32 v4, v4\n\t"
    "ds_read_b128 v[160:163], v59 offset:4096\n\t"
    "v_mfma_f32_16x16x32_bf16 v[12:15], a[124:127], a[92:95], v[12:15]\n\t"
    "v_exp_f32 v5, v5\n\t"
    "ds_read_b128 v[164:167], v60 offset:5120\n\t"
    "s_waitcnt lgkmcnt(3)\n\t"
    "v_mfma_f32_16x16x32_bf16 v[192:195], a[128:131], v[16:19], v[192:195]\n\t"
    "v_exp_f32 v6, v6\n\t"
    "ds_read_b128 v[168:171], v59 offset:6144\n\t"
    "v_mfma_f32_16x16x32_bf16 v[196:199], a[132:135], v[16:19], v[196:199]\n\t"
    "v_exp_f32 v7, v7\n\t"
    "ds_read_b128 v[172:175], v60 offset:7168\n\t"
    "v_mfma_f32_16x16x32_bf16 v[200:203], a[136:139], v[16:19], v[200:203]\n\t"
    "v_exp_f32 v8, v8\n\t"
    "v_mfma_f32_16x16x32_bf16 v[204:207], a[140:143], v[16:19], v[204:207]\n\t"
    "v_exp_f32 v9, v9\n\t"
    "v_mfma_f32_16x16x32_bf16 v[208:211], a[144:147], v[16:19], v[208:211]\n\t"
    "v_exp_f32 v10, v10\n\t"
    "v_mfma_f32_16x16x32_bf16 v[212:215], a[148:151], v[16:19], v[212:215]\n\t"
    "v_exp_f32 v11, v11\n\t"
    "v_mfma_f32_16x16x32_bf16 v[216:219], a[152:155], v[16:19], v[216:219]\n\t"
    "v_exp_f32 v12, v12\n\t"
    "v_mfma_f32_16x16x32_bf16 v[220:223], a[156:159], v[16:19], v[220:223]\n\t"
    "v_exp_f32 v13, v13\n\t"
    "v_mfma_f32_16x16x32_bf16 a[176:179], a[184:187], v[16:19], a[176:179]\n\t"
    "v_cvt_pk_bf16_f32 v16, v0, v1\n\t"
    "v_cvt_pk_bf16_f32 v17, v2, v3\n\t"
    "s_waitcnt lgkmcnt(1)\n\t"
    "buffer_store_dwordx4 v[160:163], v54, s[48:51], s31 offen nt\n\t"
    "v_mfma_f32_16x16x32_bf16 v[224:227], a[128:131], v[20:23], v[224:227]\n\t"
    "ds_read_b64_tr_b16 a[128:129], v50 offset:24576\n\t"
    "ds_read_b64_tr_b16 a[130:131], v50 offset:28672\n\t"
    "v_mfma_f32_16x16x32_bf16 v[228:231], a[132:135], v[20:23], v[228:231]\n\t"
    "ds_read_b64_tr_b16 a[132:133], v50 offset:24832\n\t"
    "ds_read_b64_tr_b16 a[134:135], v50 offset:28928\n\t"
    "v_mfma_f32_16x16x32_bf16 v[232:235], a[136:139], v[20:23], v[232:235]\n\t"
    "ds_read_b64_tr_b16 a[136:137], v50 offset:25088\n\t"
    "ds_read_b64_tr_b16 a[138:139], v50 offset:29184\n\t"
    "v_mfma_f32_16x16x32_bf16 v[236:239], a[140:143], v[20:23], v[236:239]\n\t"
    "ds_read_b64_tr_b16 a[140:141], v50 offset:25344\n\t"
    "ds_read_b64_tr_b16 a[142:143], v50 offset:29440\n\t"
    "v_mfma_f32_16x16x32_bf16 v[240:243], a[144:147], v[20:23], v[240:243]\n\t"
    "ds_read_b64_tr_b16 a[144:145], v50 offset:25600\n\t"
    "ds_read_b64_tr_b16 a[146:147], v50 offset:29696\n\t"
    "v_mfma_f32_16x16x32_bf16 v[244:247], a[148:151], v[20:23], v[244:247]\n\t"
    "ds_read_b64_tr_b16 a[148:149], v50 offset:25856\n\t"
    "ds_read_b64_tr_b16 a[150:151], v50 offset:29952\n\t"
    "v_mfma_f32_16x16x32_bf16 v[248:251], a[152:155], v[20:23], v[248:251]\n\t"
    "ds_read_b64_tr_b16 a[152:153], v50 offset:26112\n\t"
    "ds_read_b64_tr_b16 a[154:155], v50 offset:30208\n\t"
    "v_mfma_f32_16x16x32_bf16 v[252:255], a[156:159], v[20:23], v[252:255]\n\t"
    "ds_read_b64_tr_b16 a[156:157], v50 offset:26368\n\t"
    "ds_read_b64_tr_b16 a[158:159], v50 offset:30464\n\t"
    "v_cvt_pk_bf16_f32 v18, v4, v5\n\t"
    "v_cvt_pk_bf16_f32 v19, v6, v7\n\t"
    "v_mfma_f32_16x16x32_bf16 a[180:183], a[184:187], v[20:23], a[180:183]\n\t"
    "v_exp_f32 v14, v14\n\t"
    "s_waitcnt lgkmcnt(14)\n\t"
    "v_mfma_f32_16x16x32_bf16 v[192:195], a[128:131], v[16:19], v[192:195]\n\t"
    "v_exp_f32 v15, v15\n\t"
    "buffer_store_dwordx4 v[164:167], v54, s[48:51], s31 offen offset:1024 nt\n\t"
    "s_waitcnt lgkmcnt(10)\n\t"
    "v_mfma_f32_16x16x32_bf16 v[196:199], a[132:135], v[16:19], v[196:199]\n\t"
    "v_cvt_pk_bf16_f32 v20, v8, v9\n\t"
    "v_cvt_pk_bf16_f32 v21, v10, v11\n\t"
    "v_mfma_f32_16x16x32_bf16 v[200:203], a[136:139], v[16:19], v[200:203]\n\t"
    "v_cvt_pk_bf16_f32 v22, v12, v13\n\t"
    "v_cvt_pk_bf16_f32 v23, v14, v15\n\t"
    "s_waitcnt lgkmcnt(6)\n\t"
    "v_mfma_f32_16x16x32_bf16 v[204:207], a[140:143], v[16:19], v[204:207]\n\t"
    "buffer_store_dwordx4 v[168:171], v54, s[48:51], s31 offen offset:2048 nt\n\t"
    "v_mfma_f32_16x16x32_bf16 v[208:211], a[144:147], v[16:19], v[208:211]\n\t"
    "s_waitcnt lgkmcnt(2)\n\t"
    "v_mfma_f32_16x16x32_bf16 v[212:215], a[148:151], v[16:19], v[212:215]\n\t"
    "v_mfma_f32_16x16x32_bf16 v[216:219], a[152:155], v[16:19], v[216:219]\n\t"
    "s_waitcnt lgkmcnt(0)\n\t"
    "v_mfma_f32_16x16x32_bf16 v[220:223], a[156:159], v[16:19], v[220:223]\n\t"
    "v_mfma_f32_16x16x32_bf16 a[176:179], a[184:187], v[16:19], a[176:179]\n\t"
    "v_mfma_f32_16x16x32_bf16 v[224:227], a[128:131], v[20:23], v[224:227]\n\t"
    "v_mfma_f32_16x16x32_bf16 v[228:231], a[132:135], v[20:23], v[228:231]\n\t"
    "v_mfma_f32_16x16x32_bf16 v[232:235], a[136:139], v[20:23], v[232:235]\n\t"
    "buffer_store_dwordx4 v[172:175], v54, s[48:51], s31 offen offset:3072 nt\n\t"
    "v_mfma_f32_16x16x32_bf16 v[236:239], a[140:143], v[20:23], v[236:239]\n\t"
    "v_mfma_f32_16x16x32_bf16 v[240:243], a[144:147], v[20:23], v[240:243]\n\t"
    "v_mfma_f32_16x16x32_bf16 v[244:247], a[148:151], v[20:23], v[244:247]\n\t"
    "v_mfma_f32_16x16x32_bf16 v[248:251], a[152:155], v[20:23], v[248:251]\n\t"
    "v_mfma_f32_16x16x32_bf16 v[252:255], a[156:159], v[20:23], v[252:255]\n\t"
    "v_mfma_f32_16x16x32_bf16 a[180:183], a[184:187], v[20:23], a[180:183]\n\t"
    "v_accvgpr_read_b32 v61, a176\n\t"
    "v_rcp_f32 v62, v61\n\t"
    "s_add_u32 s75, s55, 16384\n\t"
    "v_cmp_nle_f32_e64 s[68:69], v61, s72\n\t"
    "s_nop 3\n\t"
    "v_accvgpr_read_b32 v63, a180\n\t"
    "v_rcp_f32 v52, v63\n\t"
    "s_add_u32 s31, s55, 20480\n\t"
    "v_cmp_nle_f32_e64 s[70:71], v63, s72\n\t"
    "v_pk_mul_f32 v[192:193], v[192:193], v[62:63] op_sel_hi:[1,0]\n\t"
    "v_pk_mul_f32 v[194:195], v[194:195], v[62:63] op_sel_hi:[1,0]\n\t"
    "v_cvt_pk_bf16_f32 v192, v192, v193\n\t"
    "v_cvt_pk_bf16_f32 v193, v194, v195\n\t"
    "ds_write_b64 v55, v[192:193] offset:0\n\t"
    "v_pk_mul_f32 v[196:197], v[196:197], v[62:63] op_sel_hi:[1,0]\n\t"
    "v_pk_mul_f32 v[198:199], v[198:199], v[62:63] op_sel_hi:[1,0]\n\t"
    "v_cvt_pk_bf16_f32 v196, v196, v197\n\t"
    "v_cvt_pk_bf16_f32 v197, v198, v199\n\t"
    "ds_write_b64 v56, v[196:197] offset:0\n\t"
    "v_pk_mul_f32 v[200:201], v[200:201], v[62:63] op_sel_hi:[1,0]\n\t"
    "v_pk_mul_f32 v[202:203], v[202:203], v[62:63] op_sel_hi:[1,0]\n\t"
    "v_cvt_pk_bf16_f32 v200, v200, v201\n\t"
    "v_cvt_pk_bf16_f32 v201, v202, v203\n\t"
    "ds_write_b64 v57, v[200:201] offset:0\n\t"
    "v_pk_mul_f32 v[204:205], v[204:205], v[62:63] op_sel_hi:[1,0]\n\t"
    "v_pk_mul_f32 v[206:207], v[206:207], v[62:63] op_sel_hi:[1,0]\n\t"
    "v_cvt_pk_bf16_f32 v204, v204, v205\n\t"
    "v_cvt_pk_bf16_f32 v205, v206, v207\n\t"
    "ds_write_b64 v58, v[204:205] offset:0\n\t"
    "v_pk_mul_f32 v[208:209], v[208:209], v[62:63] op_sel_hi:[1,0]\n\t"
    "v_pk_mul_f32 v[210:211], v[210:211], v[62:63] op_sel_hi:[1,0]\n\t"
    "v_cvt_pk_bf16_f32 v208, v208, v209\n\t"
    "v_cvt_pk_bf16_f32 v209, v210, v211\n\t"
    "ds_write_b64 v55, v[208:209] offset:128\n\t"
    "v_pk_mul_f32 v[212:213], v[212:213], v[62:63] op_sel_hi:[1,0]\n\t"
    "v_pk_mul_f32 v[214:215], v[214:215], v[62:63] op_sel_hi:[1,0]\n\t"
    "v_cvt_pk_bf16_f32 v212, v212, v213\n\t"
    "v_cvt_pk_bf16_f32 v213, v214, v215\n\t"
    "ds_write_b64 v56, v[212:213] offset:128\n\t"
    "v_pk_mul_f32 v[216:217], v[216:217], v[62:63] op_sel_hi:[1,0]\n\t"
    "v_pk_mul_f32 v[218:219], v[218:219], v[62:63] op_sel_hi:[1,0]\n\t"
    "v_cvt_pk_bf16_f32 v216, v216, v217\n\t"
    "v_cvt_pk_bf16_f32 v217, v218, v219\n\t"
    "ds_write_b64 v57, v[216:217] offset:128\n\t"
    "v_pk_mul_f32 v[220:221], v[220:221], v[62:63] op_sel_hi:[1,0]\n\t"
    "v_pk_mul_f32 v[222:223], v[222:223], v[62:63] op_sel_hi:[1,0]\n\t"
    "v_cvt_pk_bf16_f32 v220, v220, v221\n\t"
    "v_cvt_pk_bf16_f32 v221, v222, v223\n\t"
    "ds_write_b64 v58, v[220:221] offset:128\n\t"
    "ds_read_b128 v[192:195], v59 offset:0\n\t"
    "ds_read_b128 v[196:199], v60 offset:1024\n\t"
    "ds_read_b128 v[200:203], v59 offset:2048\n\t"
    "ds_read_b128 v[204:207], v60 offset:3072\n\t"
    "v_pk_mul_f32 v[224:225], v[224:225], v[52:53] op_sel_hi:[1,0]\n\t"
    "v_pk_mul_f32 v[226:227], v[226:227], v[52:53] op_sel_hi:[1,0]\n\t"
    "v_cvt_pk_bf16_f32 v224, v224, v225\n\t"
    "v_cvt_pk_bf16_f32 v225, v226, v227\n\t"
    "ds_write_b64 v55, v[224:225] offset:4096\n\t"
    "v_pk_mul_f32 v[228:229], v[228:229], v[52:53] op_sel_hi:[1,0]\n\t"
    "v_pk_mul_f32 v[230:231], v[230:231], v[52:53] op_sel_hi:[1,0]\n\t"
    "v_cvt_pk_bf16_f32 v228, v228, v229\n\t"
    "v_cvt_pk_bf16_f32 v229, v230, v231\n\t"
    "ds_write_b64 v56, v[228:229] offset:4096\n\t"
    "v_pk_mul_f32 v[232:233], v[232:233], v[52:53] op_sel_hi:[1,0]\n\t"
    "v_pk_mul_f32 v[234:235], v[234:235], v[52:53] op_sel_hi:[1,0]\n\t"
    "v_cvt_pk_bf16_f32 v232, v232, v233\n\t"
    "v_cvt_pk_bf16_f32 v233, v234, v235\n\t"
    "ds_write_b64 v57, v[232:233] offset:4096\n\t"
    "v_pk_mul_f32 v[236:237], v[236:237], v[52:53] op_sel_hi:[1,0]\n\t"
    "v_pk_mul_f32 v[238:239], v[238:239], v[52:53] op_sel_hi:[1,0]\n\t"
    "v_cvt_pk_bf16_f32 v236, v236, v237\n\t"
    "v_cvt_pk_bf16_f32 v237, v238, v239\n\t"
    "ds_write_b64 v58, v[236:237] offset:4096\n\t"
    "v_pk_mul_f32 v[240:241], v[240:241], v[52:53] op_sel_hi:[1,0]\n\t"
    "v_pk_mul_f32 v[242:243], v[242:243], v[52:53] op_sel_hi:[1,0]\n\t"
    "v_cvt_pk_bf16_f32 v240, v240, v241\n\t"
    "v_cvt_pk_bf16_f32 v241, v242, v243\n\t"
    "ds_write_b64 v55, v[240:241] offset:4224\n\t"
    "v_pk_mul_f32 v[244:245], v[244:245], v[52:53] op_sel_hi:[1,0]\n\t"
    "v_pk_mul_f32 v[246:247], v[246:247], v[52:53] op_sel_hi:[1,0]\n\t"
    "v_cvt_pk_bf16_f32 v244, v244, v245\n\t"
    "v_cvt_pk_bf16_f32 v245, v246, v247\n\t"
    "ds_write_b64 v56, v[244:245] offset:4224\n\t"
    "v_pk_mul_f32 v[248:249], v[248:249], v[52:53] op_sel_hi:[1,0]\n\t"
    "v_pk_mul_f32 v[250:251], v[250:251], v[52:53] op_sel_hi:[1,0]\n\t"
    "v_cvt_pk_bf16_f32 v248, v248, v249\n\t"
    "v_cvt_pk_bf16_f32 v249, v250, v251\n\t"
    "ds_write_b64 v57, v[248:249] offset:4224\n\t"
    "v_pk_mul_f32 v[252:253], v[252:253], v[52:53] op_sel_hi:[1,0]\n\t"
    "v_pk_mul_f32 v[254:255], v[254:255], v[52:53] op_sel_hi:[1,0]\n\t"
    "v_cvt_pk_bf16_f32 v252, v252, v253\n\t"
    "v_cvt_pk_bf16_f32 v253, v254, v255\n\t"
    "ds_write_b64 v58, v[252:253] offset:4224\n\t"
    "ds_read_b128 v[224:227], v59 offset:4096\n\t"
    "ds_read_b128 v[228:231], v60 offset:5120\n\t"
    "ds_read_b128 v[232:235], v59 offset:6144\n\t"
    "ds_read_b128 v[236:239], v60 offset:7168\n\t"
    "s_waitcnt lgkmcnt(10)\n\t"
    "buffer_store_dwordx4 v[192:195], v54, s[48:51], s75 offen nt\n\t"
    "buffer_store_dwordx4 v[196:199], v54, s[48:51], s75 offen offset:1024 nt\n\t"
    "buffer_store_dwordx4 v[200:203], v54, s[48:51], s75 offen offset:2048 nt\n\t"
    "buffer_store_dwordx4 v[204:207], v54, s[48:51], s75 offen offset:3072 nt\n\t"
    "s_waitcnt lgkmcnt(3)\n\t"
    "buffer_store_dwordx4 v[224:227], v54, s[48:51], s31 offen nt\n\t"
    "s_waitcnt lgkmcnt(2)\n\t"
    "buffer_store_dwordx4 v[228:231], v54, s[48:51], s31 offen offset:1024 nt\n\t"
    "s_waitcnt lgkmcnt(1)\n\t"
    "buffer_store_dwordx4 v[232:235], v54, s[48:51], s31 offen offset:2048 nt\n\t"
    "s_waitcnt lgkmcnt(0)\n\t"
    "buffer_store_dwordx4 v[236:239], v54, s[48:51], s31 offen offset:3072 nt\n\t"
    "s_mov_b32 %[m0], s60\n\t"
    "s_mov_b32 %[m1], s62\n\t"
    "s_mov_b32 %[m2], s64\n\t"
    "s_mov_b32 %[m3], s66\n\t"
    "s_mov_b32 %[m4], s68\n\t"
    "s_mov_b32 %[m5], s70\n\t"
    : [m0] "=s"(m0), [m1] "=s"(m1), [m2] "=s"(m2), [m3] "=s"(m3), [m4] "=s"(m4), [m5] "=s"(m5)
    : [qlo] "s"((unsigned)qp), [qhi] "s"((unsigned)(qp >> 32)), [klo] "s"((unsigned)kp), [khi] "s"((unsigned)(kp >> 32)),
      [vlo] "s"((unsigned)vp), [vhi] "s"((unsigned)(vp >> 32)), [olo] "s"((unsigned)op), [ohi] "s"((unsigned)(op >> 32)),
      [w] "s"(w), [lds] "s"(ldsb), [xi] "s"(blockIdx.x >> 3)
    : "memory", "vcc", "scc", "s31", "s32", "s33", "s34", "s35", "s36", "s37", "s38", "s39", "s40", "s41", "s42", "s43", "s44", "s45", "s46", "s47", "s48", "s49", "s50", "s51", "s52", "s53", "s54", "s55", "s56", "s57", "s58", "s59", "s60", "s61", "s62", "s63", "s64", "s65", "s66", "s67", "s68", "s69", "s70", "s71", "s72", "s73", "s74", "s75",
      "v0", "v1", "v2", "v3", "v4", "v5", "v6", "v7", "v8", "v9", "v10", "v11", "v12", "v13", "v14", "v15", "v16", "v17", "v18", "v19", "v20", "v21", "v22", "v23", "v24", "v25", "v26", "v27", "v28", "v29", "v30", "v31", "v32", "v33", "v34", "v35", "v36", "v37", "v38", "v39", "v40", "v41", "v42", "v43", "v44", "v45", "v46", "v47", "v48", "v49", "v50", "v51", "v52", "v53", "v54", "v55", "v56", "v57", "v58", "v59", "v60", "v61", "v62", "v63", "v64", "v65", "v66", "v67", "v68", "v69", "v70", "v71", "v72", "v73", "v74", "v75", "v76", "v77", "v78", "v79", "v80", "v81", "v82", "v83", "v84", "v85", "v86", "v87", "v88", "v89", "v90", "v91", "v92", "v93", "v94", "v95", "v96", "v97", "v98", "v99", "v100", "v101", "v102", "v103", "v104", "v105", "v106", "v107", "v108", "v109", "v110", "v111", "v112", "v113", "v114", "v115", "v116", "v117", "v118", "v119", "v120", "v121", "v122", "v123", "v124", "v125", "v126", "v127", "v128", "v129", "v130", "v131", "v132", "v133", "v134", "v135", "v136", "v137", "v138", "v139", "v140", "v141", "v142", "v143", "v144", "v145", "v146", "v147", "v148", "v149", "v150", "v151", "v152", "v153", "v154", "v155", "v156", "v157", "v158", "v159", "v160", "v161", "v162", "v163", "v164", "v165", "v166", "v167", "v168", "v169", "v170", "v171", "v172", "v173", "v174", "v175", "v176", "v177", "v178", "v179", "v180", "v181", "v182", "v183", "v184", "v185", "v186", "v187", "v188", "v189", "v190", "v191", "v192", "v193", "v194", "v195", "v196", "v197", "v198", "v199", "v200", "v201", "v202", "v203", "v204", "v205", "v206", "v207", "v208", "v209", "v210", "v211", "v212", "v213", "v214", "v215", "v216", "v217", "v218", "v219", "v220", "v221", "v222", "v223", "v224", "v225", "v226", "v227", "v228", "v229", "v230", "v231", "v232", "v233", "v234", "v235", "v236", "v237", "v238", "v239", "v240", "v241", "v242", "v243", "v244", "v245", "v246", "v247", "v248", "v249", "v250", "v251", "v252", "v253", "v254", "v255",
      "a0", "a1", "a2", "a3", "a4", "a5", "a6", "a7", "a8", "a9", "a10", "a11", "a12", "a13", "a14", "a15", "a16", "a17", "a18", "a19", "a20", "a21", "a22", "a23", "a24", "a25", "a26", "a27", "a28", "a29", "a30", "a31", "a32", "a33", "a34", "a35", "a36", "a37", "a38", "a39", "a40", "a41", "a42", "a43", "a44", "a45", "a46", "a47", "a48", "a49", "a50", "a51", "a52", "a53", "a54", "a55", "a56", "a57", "a58", "a59", "a60", "a61", "a62", "a63", "a64", "a65", "a66", "a67", "a68", "a69", "a70", "a71", "a72", "a73", "a74", "a75", "a76", "a77", "a78", "a79", "a80", "a81", "a82", "a83", "a84", "a85", "a86", "a87", "a88", "a89", "a90", "a91", "a92", "a93", "a94", "a95", "a96", "a97", "a98", "a99", "a100", "a101", "a102", "a103", "a104", "a105", "a106", "a107", "a108", "a109", "a110", "a111", "a112", "a113", "a114", "a115", "a116", "a117", "a118", "a119", "a120", "a121", "a122", "a123", "a124", "a125", "a126", "a127", "a128", "a129", "a130", "a131", "a132", "a133", "a134", "a135", "a136", "a137", "a138", "a139", "a140", "a141", "a142", "a143", "a144", "a145", "a146", "a147", "a148", "a149", "a150", "a151", "a152", "a153", "a154", "a155", "a156", "a157", "a158", "a159", "a160", "a161", "a162", "a163", "a164", "a165", "a166", "a167", "a168", "a169", "a170", "a171", "a172", "a173", "a174", "a175", "a176", "a177", "a178", "a179", "a180", "a181", "a182", "a183", "a184", "a185", "a186", "a187", "a188", "a189", "a190", "a191", "a192", "a193", "a194", "a195", "a196", "a197", "a198", "a199", "a200", "a201", "a202", "a203", "a204", "a205", "a206", "a207", "a208", "a209", "a210", "a211", "a212", "a213", "a214", "a215", "a216", "a217", "a218", "a219", "a220", "a221", "a222", "a223", "a224", "a225", "a226", "a227", "a228", "a229", "a230", "a231", "a232", "a233", "a234", "a235", "a236", "a237", "a238", "a239", "a240", "a241", "a242", "a243", "a244", "a245", "a246", "a247", "a248", "a249", "a250", "a251", "a252", "a253", "a254", "a255");
  const unsigned long long bad = (unsigned long long)((m0 & 0xffffu) | (m1 << 16)) | ((unsigned long long)((m2 & 0xffffu) | (m3 << 16)) << 32);
  const unsigned bad2 = (m4 & 0xffffu) | (m5 << 16);
  if (__builtin_expect((bad | bad2) != 0, 0)) {
    asm volatile("s_waitcnt vmcnt(0)" ::: "memory");
    unsigned lane; asm volatile("v_mbcnt_lo_u32_b32 %0, -1, 0\n\tv_mbcnt_hi_u32_b32 %0, -1, %0" : "=v"(lane));
    for (int r = 0; r < 96; ++r) {
      const bool f = r < 64 ? ((bad >> r) & 1ull) : ((bad2 >> (r - 64)) & 1u);
      if (f) { const size_t row = (size_t)w * 96 + r; slow_row(Q + hb + row * kD, K + hb, V + hb, O + hb + row * kD, lane); }
    }
  }
}

extern "C" void kernel_launch(void* const* d_in, const int* in_sizes, int n_in, void* d_out, int out_size, void* d_ws, size_t ws_size,
                              hipStream_t stream) {
  (void)in_sizes; (void)n_in; (void)out_size; (void)d_ws; (void)ws_size;
  (void)hipFuncSetAttribute((const void*)mha_fwd_kernel, hipFuncAttributeMaxDynamicSharedMemorySize, kLds);
  hipLaunchKernelGGL(mha_fwd_kernel, dim3(kHeads), dim3(256), kLds, stream, (const unsigned short*)d_in[0], (const unsigned short*)d_in[1],
                     (const unsigned short*)d_in[2], (unsigned short*)d_out
  );
}
```
